```python
import torch

def _softmax(x, axis=-1):
    return torch.softmax(x, dim=axis)
def _gelu(x):
    return torch.nn.functional.gelu(x, approximate="tanh")
def _silu(x):
    return torch.nn.functional.silu(x)
def _sigmoid(x):
    return torch.sigmoid(x)
def _layernorm(x, axis=-1, eps=1e-5):
    m = x.mean(dim=axis, keepdim=True)
    v = x.var(dim=axis, keepdim=True, unbiased=False)
    return (x - m) / torch.sqrt(v + eps)
def _scatter_add(tgt, idx, src, axis=0):
    return tgt.clone().index_add_(axis, idx, src)
def _segment_sum(src, seg, n):
    out = torch.zeros((n,) + src.shape[1:], dtype=src.dtype)
    return out.index_add_(0, seg, src)
def _pad(x, pw):
    flat = tuple(v for pair in reversed(pw) for v in pair)
    return torch.nn.functional.pad(x, flat)

kernel_name = 'concat_heads__linear_kernel_hedgehog__sliding_local_dilated__prompt'

def setup_inputs():

    _w = (lambda a: (lambda t: t.to(torch.bfloat16) if t.is_floating_point() else t)(torch.as_tensor(a)))
    def _A_setup():
        B, S, D, H = 1, 2048, 576, 32
        Dh = D // H
        return {
            "x": _w((lambda *s: torch.randn(*s, dtype=torch.bfloat16) * 0.2)(B, S, D)),
            "wqkv": _w((lambda *s: torch.randn(*s, dtype=torch.bfloat16) * 0.8)(D, 3 * D)),
            "w_phi": _w((lambda *s: torch.randn(*s, dtype=torch.bfloat16) * 0.8)(H, Dh, Dh) * (Dh ** -0.5)),
            "wo": _w((lambda *s: torch.randn(*s, dtype=torch.bfloat16) * 0.2)(D, D)),
        }
    _A_in = _A_setup()

    def _B_setup():
        B, S, D = 1, 2048, 576
        return {
            "x": _w((lambda *s: torch.randn(*s, dtype=torch.bfloat16) * 0.2)(B, S, D)),
            "wqkv": _w((lambda *s: torch.randn(*s, dtype=torch.bfloat16) * 0.2)(D, 3 * D)),
            "wo": _w((lambda *s: torch.randn(*s, dtype=torch.bfloat16) * 0.05)(D, D)),
        }
    _B_in = _B_setup()

    return {
        "A_x": _A_in["x"],
        "A_wqkv": _A_in["wqkv"],
        "A_w_phi": _A_in["w_phi"],
        "A_wo": _A_in["wo"],
        "B_x": _B_in["x"],
        "B_wqkv": _B_in["wqkv"],
        "B_wo": _B_in["wo"],
    }

def reference(A_x, A_wqkv, A_w_phi, A_wo, B_x, B_wqkv, B_wo):
    def _A_ref(x, wqkv, w_phi, wo):
        B, S, D = x.shape
        H = 32; Dh = D // H
        qkv = (lambda x, *d: x.permute(*d))((x @ wqkv).reshape(B, S, 3, H, Dh), 2, 0, 3, 1, 4)
        q, k, v = qkv[0], qkv[1], qkv[2]
        pq = torch.einsum("bhsd,hdf->bhsf", q, w_phi)
        pk = torch.einsum("bhsd,hdf->bhsf", k, w_phi)
        phi_q = torch.concatenate([_softmax(pq, axis=-1), _softmax(-pq, axis=-1)], axis=-1)
        phi_k = torch.concatenate([_softmax(pk, axis=-1), _softmax(-pk, axis=-1)], axis=-1)
        kv = torch.einsum("bhsf,bhsd->bhfd", phi_k, v)
        z  = torch.einsum("bhsf->bhf", phi_k)
        num = torch.einsum("bhqf,bhfd->bhqd", phi_q, kv)
        den = torch.einsum("bhqf,bhf->bhq", phi_q, z)[..., None] + 1e-6
        o = num / den
        return (lambda x, *d: x.permute(*d))(o, 0, 2, 1, 3).reshape(B, S, D) @ wo

    def _B_ref(x, wqkv, wo):
        B, S, D = x.shape
        H = 32; Dh = D // H
        W, dil = 8, 2
        qkv = (lambda x, *d: x.permute(*d))((x @ wqkv).reshape(B, S, 3, H, Dh), 2, 0, 3, 1, 4)
        q, k, v = qkv[0], qkv[1], qkv[2]
        scale = Dh ** -0.5
        pos = torch.arange(S)
        off = torch.arange(W) * dil
        idx = pos[:, None] - off[None, :]
        mask = idx >= 0
        idx_c = torch.where(mask, idx, 0).reshape(-1)
        kg = k[:, :, idx_c, :].reshape(B, H, S, W, Dh)
        vg = v[:, :, idx_c, :].reshape(B, H, S, W, Dh)
        logits = torch.einsum("bhsd,bhswd->bhsw", q, kg) * scale
        logits = torch.where(mask[None, None, :, :], logits, -1e9)
        attn = _softmax(logits, axis=-1)
        o = torch.einsum("bhsw,bhswd->bhsd", attn, vg)
        return (lambda x, *d: x.permute(*d))(o, 0, 2, 1, 3).reshape(B, S, D) @ wo

    a = _A_ref(A_x, A_wqkv, A_w_phi, A_wo)
    b = _B_ref(B_x, B_wqkv, B_wo)
    return torch.concatenate([a, b], axis=-1)
```

```cpp
#include <hip/hip_runtime.h>
#include <hip/hip_bf16.h>
typedef __hip_bfloat16 bf16;
__device__ __forceinline__ float b2f(bf16 v){ return __bfloat162float(v); }
__device__ __forceinline__ bf16 f2b(float v){ return __float2bfloat16(v); }
__device__ __forceinline__ float rb(float v){ return __bfloat162float(__float2bfloat16(v)); }

constexpr int S=2048, D=576, H=32, DH=18, N3=1728;

__global__ void gemm_naive(const bf16* __restrict__ A, const bf16* __restrict__ B, bf16* __restrict__ C, int M, int N, int K, int ldc){
  __shared__ float As[16][17], Bs[16][17];
  int tx=threadIdx.x, ty=threadIdx.y;
  int row=blockIdx.y*16+ty, col=blockIdx.x*16+tx;
  float acc=0.f;
  for(int k0=0;k0<K;k0+=16){
    As[ty][tx]=b2f(A[(long)row*K+k0+tx]);
    Bs[ty][tx]=b2f(B[(long)(k0+ty)*N+col]);
    __syncthreads();
    for(int k=0;k<16;++k) acc+=As[ty][k]*Bs[k][tx];
    __syncthreads();
  }
  C[(long)row*ldc+col]=f2b(acc);
}

__global__ __launch_bounds__(256) void featA(const bf16* __restrict__ qkv, const bf16* __restrict__ wphi, bf16* __restrict__ phiq, bf16* __restrict__ phik){
  int idx=blockIdx.x*blockDim.x+threadIdx.x;
  if(idx>=S*H*2) return;
  int which=idx&1, h=(idx>>1)%H, s=idx/(2*H);
  const bf16* src=qkv+(long)s*N3+which*D+h*DH;
  float p[DH];
  float mx=-INFINITY, mn=INFINITY;
  #pragma unroll
  for(int f=0;f<DH;++f){ float a=0.f;
    #pragma unroll
    for(int d=0;d<DH;++d) a+=b2f(src[d])*b2f(wphi[(h*DH+d)*DH+f]);
    p[f]=rb(a); mx=fmaxf(mx,p[f]); mn=fminf(mn,p[f]); }
  float s1=0.f,s2=0.f;
  #pragma unroll
  for(int f=0;f<DH;++f){ s1+=__expf(p[f]-mx); s2+=__expf(mn-p[f]); }
  bf16* dst=(which?phik:phiq)+((long)h*S+s)*36;
  #pragma unroll
  for(int f=0;f<DH;++f){ dst[f]=f2b(__expf(p[f]-mx)/s1); dst[18+f]=f2b(__expf(mn-p[f])/s2); }
}

__global__ void kvA(const bf16* __restrict__ qkv, const bf16* __restrict__ phik, float* __restrict__ kv){
  int h=blockIdx.x/36, f=blockIdx.x%36;
  float acc[19];
  #pragma unroll
  for(int d=0;d<19;++d) acc[d]=0.f;
  for(int s=threadIdx.x;s<S;s+=256){
    float pk=b2f(phik[((long)h*S+s)*36+f]);
    const bf16* v=qkv+(long)s*N3+2*D+h*DH;
    #pragma unroll
    for(int d=0;d<18;++d) acc[d]+=pk*b2f(v[d]);
    acc[18]+=pk;
  }
  __shared__ float red[256];
  #pragma unroll
  for(int d=0;d<19;++d){
    red[threadIdx.x]=acc[d]; __syncthreads();
    for(int o=128;o>0;o>>=1){ if(threadIdx.x<o) red[threadIdx.x]+=red[threadIdx.x+o]; __syncthreads(); }
    if(threadIdx.x==0) kv[(h*36+f)*19+d]=rb(red[0]);
    __syncthreads();
  }
}

__global__ void oA(const bf16* __restrict__ phiq, const float* __restrict__ kv, bf16* __restrict__ o){
  int idx=blockIdx.x*blockDim.x+threadIdx.x; if(idx>=S*H) return;
  int h=idx%H, s=idx/H;
  float num[19];
  #pragma unroll
  for(int d=0;d<19;++d) num[d]=0.f;
  for(int f=0;f<36;++f){ float pq=b2f(phiq[((long)h*S+s)*36+f]);
    #pragma unroll
    for(int d=0;d<19;++d) num[d]+=pq*kv[(h*36+f)*19+d]; }
  float den=rb(rb(num[18])+1e-6f);
  #pragma unroll
  for(int d=0;d<18;++d) o[(long)s*D+h*DH+d]=f2b(rb(num[d])/den);
}

__global__ void attnB(const bf16* __restrict__ qkv, bf16* __restrict__ o){
  int idx=blockIdx.x*blockDim.x+threadIdx.x; if(idx>=S*H) return;
  int h=idx%H, s=idx/H;
  float q[DH];
  #pragma unroll
  for(int d=0;d<DH;++d) q[d]=b2f(qkv[(long)s*N3+h*DH+d]);
  float lg[8]; float mx=-INFINITY;
  const float scale=0.23570226039551584f;
  #pragma unroll
  for(int w=0;w<8;++w){ int j=s-2*w;
    if(j>=0){ float a=0.f;
      #pragma unroll
      for(int d=0;d<DH;++d) a+=q[d]*b2f(qkv[(long)j*N3+D+h*DH+d]);
      lg[w]=rb(rb(a)*scale);} else lg[w]=-1e9f;
    mx=fmaxf(mx,lg[w]); }
  float sum=0.f;
  #pragma unroll
  for(int w=0;w<8;++w){ lg[w]=__expf(lg[w]-mx); sum+=lg[w]; }
  float acc[DH];
  #pragma unroll
  for(int d=0;d<DH;++d) acc[d]=0.f;
  #pragma unroll
  for(int w=0;w<8;++w){ int j=s-2*w; if(j<0) j=0; float p=rb(lg[w]/sum);
    #pragma unroll
    for(int d=0;d<DH;++d) acc[d]+=p*b2f(qkv[(long)j*N3+2*D+h*DH+d]); }
  #pragma unroll
  for(int d=0;d<DH;++d) o[(long)s*D+h*DH+d]=f2b(acc[d]);
}

extern "C" void kernel_launch(void* const* d_in, const int* in_sizes, int n_in,
                              void* d_out, int out_size, void* d_ws, size_t ws_size,
                              hipStream_t stream) {
  const bf16* Ax=(const bf16*)d_in[0]; const bf16* Awqkv=(const bf16*)d_in[1]; const bf16* Awphi=(const bf16*)d_in[2]; const bf16* Awo=(const bf16*)d_in[3];
  const bf16* Bx=(const bf16*)d_in[4]; const bf16* Bwqkv=(const bf16*)d_in[5]; const bf16* Bwo=(const bf16*)d_in[6];
  bf16* out=(bf16*)d_out;
  char* ws=(char*)d_ws;
  bf16* qkvA=(bf16*)ws; ws+=(size_t)S*N3*2;
  bf16* qkvB=(bf16*)ws; ws+=(size_t)S*N3*2;
  bf16* phiq=(bf16*)ws; ws+=(size_t)H*S*36*2;
  bf16* phik=(bf16*)ws; ws+=(size_t)H*S*36*2;
  bf16* oa=(bf16*)ws; ws+=(size_t)S*D*2;
  bf16* ob=(bf16*)ws; ws+=(size_t)S*D*2;
  float* kv=(float*)ws;
  gemm_naive<<<dim3(N3/16,S/16),dim3(16,16),0,stream>>>(Ax,Awqkv,qkvA,S,N3,D,N3);
  gemm_naive<<<dim3(N3/16,S/16),dim3(16,16),0,stream>>>(Bx,Bwqkv,qkvB,S,N3,D,N3);
  featA<<<S*H*2/256,256,0,stream>>>(qkvA,Awphi,phiq,phik);
  kvA<<<H*36,256,0,stream>>>(qkvA,phik,kv);
  oA<<<S*H/256,256,0,stream>>>(phiq,kv,oa);
  attnB<<<S*H/256,256,0,stream>>>(qkvB,ob);
  gemm_naive<<<dim3(D/16,S/16),dim3(16,16),0,stream>>>(oa,Awo,out,S,D,D,2*D);
  gemm_naive<<<dim3(D/16,S/16),dim3(16,16),0,stream>>>(ob,Bwo,out+D,S,D,D,2*D);
}
```

```cpp
#include <hip/hip_runtime.h>
#include <stdint.h>

typedef short s16x4 __attribute__((ext_vector_type(4)));
typedef __bf16 bf16x8 __attribute__((ext_vector_type(8)));
typedef __bf16 bf16x2 __attribute__((ext_vector_type(2)));
typedef float f32x4 __attribute__((ext_vector_type(4)));
typedef float f32x2 __attribute__((ext_vector_type(2)));
typedef unsigned u32x4 __attribute__((ext_vector_type(4)));
typedef unsigned u32x2 __attribute__((ext_vector_type(2)));
typedef unsigned u32x16 __attribute__((ext_vector_type(16)));
typedef unsigned long long u64;
#define LAS __attribute__((address_space(3)))
#define GAS __attribute__((address_space(1)))
#define DEV __device__ __forceinline__

constexpr int SLOT = 36864;
constexpr int L_S1 = 0, L_S2 = SLOT, L_S3 = 2 * SLOT, L_S0 = 3 * SLOT;
constexpr int L_REC = L_S3 + 9216;
constexpr int L_ZERO = L_REC + 65536;
constexpr int L_PT = L_ZERO + 64;
constexpr int L_KVT = L_PT + 3840;
constexpr int L_END = L_KVT + 5760;
static_assert(L_END <= 163840, "lds");
constexpr int LDS_BYTES = 163840;
constexpr int slot_of(int s) { return (s & 3) == 0 ? L_S0 : (s & 3) == 1 ? L_S1 : (s & 3) == 2 ? L_S2 : L_S3; }

constexpr int W_EPOCH = 0;
constexpr int W_FK = 1024;
constexpr int W_FO = 4096;
constexpr int W_KV = 65536;
constexpr int W_OB = 1048576;
constexpr int W_TOTAL = W_OB + 2 * 16 * 8 * 18432;
constexpr unsigned SENT = 0xFFFFFFFFu;
constexpr int OOB = 0x40000000;
constexpr int SPIN_CAP = 20000;

template <int N> DEV void waitv() { asm volatile("s_waitcnt vmcnt(%0)" ::"n"(N) : "memory"); }
DEV void bar() { asm volatile("s_waitcnt lgkmcnt(0)\n\ts_barrier" ::: "memory"); }

DEV unsigned pk2(float a, float b) {
  f32x2 v = {a, b};
  bf16x2 r = __builtin_convertvector(v, bf16x2);
  return __builtin_bit_cast(unsigned, r);
}
DEV float lo2f(unsigned u) { return __uint_as_float(u << 16); }
DEV float hi2f(unsigned u) { return __uint_as_float(u & 0xFFFF0000u); }
DEV bf16x8 asb(u32x4 v) { return __builtin_bit_cast(bf16x8, v); }
DEV f32x4 mfma(bf16x8 a, bf16x8 b, f32x4 c) { return __builtin_amdgcn_mfma_f32_16x16x32_bf16(a, b, c, 0, 0, 0); }
DEV float dot2(unsigned a, unsigned b, float c) {
  return __builtin_amdgcn_fdot2_f32_bf16(__builtin_bit_cast(bf16x2, a), __builtin_bit_cast(bf16x2, b), c, false);
}
DEV u32x4 lds128(LAS char* L, int off) { return *(LAS u32x4*)(L + off); }
DEV u32x2 lds64(LAS char* L, int off) { return *(LAS u32x2*)(L + off); }
DEV unsigned lds32(LAS char* L, int off) { return *(LAS unsigned*)(L + off); }
template <int STEP> DEV bf16x8 trfrag(LAS char* L, int off) {
  s16x4 a = __builtin_amdgcn_ds_read_tr16_b64_v4i16((LAS s16x4*)(L + off));
  s16x4 b = __builtin_amdgcn_ds_read_tr16_b64_v4i16((LAS s16x4*)(L + off + STEP));
  u32x2 ua = __builtin_bit_cast(u32x2, a), ub = __builtin_bit_cast(u32x2, b);
  u32x4 r = {ua.x, ua.y, ub.x, ub.y};
  return asb(r);
}
DEV void glds16(const char* base, unsigned voff, int dst) {
  asm volatile("s_mov_b32 m0, %2\n\ts_nop 0\n\tglobal_load_lds_dwordx4 %0, %1" ::"v"(voff), "s"(base), "s"(dst) : "memory", "m0");
}
template <int LIM> DEV void glds16_lt(int wid, const char* base, unsigned voff, int dst) {
  asm volatile("s_cmp_lt_u32 %3, %4\n\ts_cbranch_scc0 .Lskip%=\n\ts_mov_b32 m0, %2\n\ts_nop 0\n\tglobal_load_lds_dwordx4 %0, %1\n.Lskip%=:"
               ::"v"(voff), "s"(base), "s"(dst), "s"(wid), "n"(LIM) : "memory", "m0", "scc");
}
template <int NA, int NB> DEV void waitbar(int wid) {
  asm volatile("s_cmp_lt_u32 %0, 4\n\ts_cbranch_scc1 .Lw%=\n\ts_waitcnt vmcnt(%2)\n.Lw%=:\n\ts_waitcnt vmcnt(%1) lgkmcnt(0)\n\ts_barrier"
               ::"s"(wid), "n"(NA), "n"(NB) : "memory", "scc");
}
DEV int opq(int v) { asm volatile("" : "+v"(v)); return v; }
DEV void swap16(unsigned& a, unsigned& b) {
  auto r = __builtin_amdgcn_permlane16_swap(a, b, false, false);
  a = r[0]; b = r[1];
}
DEV void swap32(unsigned& a, unsigned& b) {
  auto r = __builtin_amdgcn_permlane32_swap(a, b, false, false);
  a = r[0]; b = r[1];
}
DEV void swap16f(float& a, float& b) {
  unsigned x = __float_as_uint(a), y = __float_as_uint(b);
  swap16(x, y);
  a = __uint_as_float(x); b = __uint_as_float(y);
}
DEV unsigned canon(unsigned x) { return x < 0xFFFFFFFEu ? x : 0xFFFFFFFEu; }
DEV unsigned umax3(unsigned a, unsigned b, unsigned c) { unsigned m = a > b ? a : b; return m > c ? m : c; }

struct Params {
  const char* x[2];
  const char* w[2];
  const char* wo[2];
  const char* wphi;
  char* out;
  char* ws;
};

DEV int kmap(int pos) {
  int pp = pos & 31;
  return (pos & 32) + 8 * (((pp >> 4) << 1) | ((pp >> 3) & 1)) + 4 * (pp & 1) + ((pp >> 1) & 3);
}

DEV void features(LAS char* L, int pta, int ptb, bool act, const u32x4 (&m)[2], const unsigned (&e)[2], unsigned (&ph)[2][9]) {
  f32x4 P[2][3];
#pragma unroll
  for (int t = 0; t < 3; ++t) {
    u32x4 T0 = lds128(L, act ? pta + t * 512 : L_ZERO);
    u32x4 T1 = {lds32(L, act ? ptb + t * 128 : L_ZERO), 0u, 0u, 0u};
#pragma unroll
    for (int mt = 0; mt < 2; ++mt) {
      f32x4 z = {0.f, 0.f, 0.f, 0.f};
      u32x4 E = {e[mt], 0u, 0u, 0u};
      z = mfma(asb(T0), asb(m[mt]), z);
      P[mt][t] = mfma(asb(T1), asb(E), z);
    }
  }
  const float LOG2E = 1.4426950408889634f;
  float p[2][9], mx[2], mn[2];
#pragma unroll
  for (int mt = 0; mt < 2; ++mt) {
#pragma unroll
    for (int i = 0; i < 4; ++i) {
      unsigned u = pk2(P[mt][i >> 1][(i & 1) * 2], P[mt][i >> 1][(i & 1) * 2 + 1]);
      p[mt][2 * i] = lo2f(u); p[mt][2 * i + 1] = hi2f(u);
    }
    p[mt][8] = lo2f(pk2(P[mt][2][0], 0.f));
    float a = p[mt][0], b = p[mt][0];
#pragma unroll
    for (int i = 1; i < 9; ++i) { a = __builtin_fmaxf(a, p[mt][i]); b = __builtin_fminf(b, p[mt][i]); }
    float a2 = a, b2 = b;
    swap16f(a, a2); swap16f(b, b2);
    mx[mt] = __builtin_fmaxf(a, a2) * LOG2E; mn[mt] = __builtin_fminf(b, b2) * LOG2E;
  }
#pragma unroll
  for (int mt = 0; mt < 2; ++mt) {
    float ep[9], en[9], sp = 0.f, sn = 0.f;
#pragma unroll
    for (int i = 0; i < 9; ++i) {
      ep[i] = __builtin_amdgcn_exp2f(__builtin_fmaf(p[mt][i], LOG2E, -mx[mt]));
      en[i] = __builtin_amdgcn_exp2f(__builtin_fmaf(p[mt][i], -LOG2E, mn[mt]));
      sp += ep[i]; sn += en[i];
    }
    float sp2 = sp, sn2 = sn;
    swap16f(sp, sp2); swap16f(sn, sn2);
    float rp = __builtin_amdgcn_rcpf(sp + sp2), rn = __builtin_amdgcn_rcpf(sn + sn2);
    float f[18];
#pragma unroll
    for (int i = 0; i < 9; ++i) { f[i] = ep[i] * rp; f[9 + i] = en[i] * rn; }
#pragma unroll
    for (int i = 0; i < 9; ++i) ph[mt][i] = pk2(f[2 * i], f[2 * i + 1]);
  }
}

template <bool ISA>
DEV void body(const Params& P, LAS char* L, int rt, int hg, u64 tag) {
  const int br = ISA ? 0 : 1;
  const int tid = threadIdx.x;
  const int wid = __builtin_amdgcn_readfirstlane(tid >> 6);
  const int lane = tid & 63;
  const int r = wid & 3, h = wid >> 2;
  const int g = lane >> 4, c = lane & 15;
  const bool five = wid < 4;
  const char* X = P.x[br];
  const char* W = P.w[br];
  const char* WO = P.wo[br];
  __amdgpu_buffer_rsrc_t wsr = __builtin_amdgcn_make_buffer_rsrc(P.ws, 0, W_TOTAL, 0x00020000);

  unsigned jo[5];
  {
#pragma unroll
    for (int i = 0; i < 5; ++i) {
      int J = wid + 8 * i;
      int rp = 8 * J + (lane >> 3);
      int cc = rp & 15;
      int row = rt * 128 + (rp & 96) + 4 * (cc >> 1) + (cc & 1) + 2 * ((rp >> 4) & 1);
      int hrow = rt * 128 - 16 + cc; hrow = hrow < 0 ? 0 : hrow;
      unsigned xo = (unsigned)((rp < 128 ? row : hrow) * 1152 + (((lane & 7) ^ ((rp >> 1) & 7)) * 16));
      int U = (J - 18) * 64 + lane; U = U < 0 ? 0 : U;
      int R = U / 9, un = U - 9 * R;
      unsigned wo = (unsigned)(kmap(R & 63) * 3456 + (1 + (R >> 6)) * 1152 + hg * 144 + un * 16);
      jo[i] = (i < 2) ? xo : (i > 2) ? wo : (wid < 2 ? xo : wo);
    }
  }
  const char* j2base = wid < 2 ? X : W;
  const int j2step = wid < 2 ? 128 : 64 * 3456;
  auto issue1 = [&](int s) {
    const int d = slot_of(s) + wid * 1024;
    glds16(X + s * 128, jo[0], d);
    glds16(X + s * 128, jo[1], d + 8192);
    glds16(j2base + s * j2step, jo[2], d + 16384);
    glds16(W + s * (64 * 3456), jo[3], d + 24576);
    glds16_lt<4>(wid, W + s * (64 * 3456), jo[4], d + 32768);
  };
  if (ISA) {
    int o = wid * 1024 + lane * 16; o = o > 2576 ? 2576 : o;
    glds16_lt<3>(wid, P.wphi + hg * 2592, o, L_KVT + wid * 1024);
  }
  issue1(0); issue1(1); issue1(2);

  const int obm = W_OB + ((br * 16 + rt) * 8 + 2 * r) * 18432 + (2 * hg + h) * 1024 + lane * 16;
  const int obr = g == 1 ? W_OB + ((br * 16 + rt) * 8 + 2 * r) * 18432 + 16384 + (2 * hg + h) * 128 + c * 8 : OOB;
  auto store_o = [&](int mt, u32x4 m, u32x2 x) {
    __builtin_amdgcn_raw_buffer_store_b128(m, wsr, obm + mt * 18432, 0, 16);
    __builtin_amdgcn_raw_buffer_store_b64(x, wsr, obr + mt * 18432, 0, 16);
  };
  int dI;
  {
    int a = (c & 7) >= 2 ? (c >> 3) * 8 + (c & 7) - 2 : -1;
    int b = c < 2 ? 6 + c : (c >= 8 && c < 14) ? 6 + c : -1;
    dI = h == 0 ? a : b;
  }
  const int kvb = W_KV + (hg * 16 + rt) * 5760 + r * 1440;
  const int kvm = dI >= 0 ? kvb + dI * 64 + (((g & 1) << 1) | (g >> 1)) * 16 : OOB;
  const int kvx = (dI >= 0 && g == 0) ? kvb + 1280 + dI * 8 : OOB;
  auto store_kv = [&](u32x4 m, u32x2 x) {
    __builtin_amdgcn_raw_buffer_store_b128(m, wsr, kvm, 0, 16);
    __builtin_amdgcn_raw_buffer_store_b64(x, wsr, kvx, 0, 16);
  };
  {
    u32x4 s4 = {SENT, SENT, SENT, SENT}; u32x2 s2 = {SENT, SENT};
    store_o(0, s4, s2); store_o(1, s4, s2);
    if (ISA) store_kv(s4, s2);
  }
  constexpr int NCLR = ISA ? 6 : 4;

  const int tq = (lane >> 2) & 3, tp = lane & 3;
  const int wrow = (16 * (g >> 1) + 8 * (g & 1) + 2 * tq) * 144;
  const int wa = wrow + ((tp == 0 ? 0 : tp == 1 ? 2 : tp == 2 ? 5 : 7) + 9 * h) * 8;
  const int wsd = wrow + (4 + 9 * h) * 8 + (tp & 1) * 9216;
  const int xa0 = (32 * r + c) * 128 + ((g ^ (c >> 1)) * 16);
  const int hoff = (r & 1) * 8 + (r >> 1) * 9216;

  u32x4 xf[18][2];
  f32x4 acc[5][2], hacc[2];
#pragma unroll
  for (int t = 0; t < 5; ++t) { acc[t][0] = f32x4{0.f, 0.f, 0.f, 0.f}; acc[t][1] = acc[t][0]; }
  hacc[0] = f32x4{0.f, 0.f, 0.f, 0.f}; hacc[1] = hacc[0];

  unsigned qo[5];
  auto qjobs = [&](bool forwo) {
#pragma unroll
    for (int i = 0; i < 5; ++i) {
      int U = (wid + 8 * i) * 64 + lane;
      int R = U / 9, un = U - 9 * R;
      int k = (R & 192) + kmap(R & 63);
      if (!forwo) qo[i] = (unsigned)(k * 3456 + hg * 144 + un * 16);
      else {
        int kk = k & 31;
        int row = 36 * (k >> 5) + kk + (kk >= 16 ? 4 : 0);
        qo[i] = (unsigned)(row * 1152 + hg * 144 + un * 16);
      }
    }
  };
  auto issue4 = [&](const char* base, int slot) {
    const int d = slot + wid * 1024;
#pragma unroll
    for (int i = 0; i < 4; ++i) glds16(base, qo[i], d + i * 8192);
    glds16_lt<4>(wid, base, qo[4], d + 32768);
  };

#pragma unroll
  for (int s = 0; s < 9; ++s) {
    if (s <= 2) waitbar<10 + NCLR, 8 + NCLR>(wid); else waitbar<10, 8>(wid);
    if (s + 3 < 9) issue1(s + 3);
    else if (s + 3 == 9) { qjobs(false); issue4(W, L_S1); }
    else if (s + 3 == 10) issue4(W + 256 * 3456, L_S2);
    else {
      glds16(W + 512 * 3456, qo[0], L_S3 + wid * 1024);
      glds16_lt<1>(wid, W + 512 * 3456, qo[1], L_S3 + 8192);
    }
    if (s == 0) {
      if (tid < 16) *(LAS unsigned*)(L + L_ZERO + tid * 4) = 0u;
      if (ISA) {
#pragma unroll
        for (int it = 0; it < 4; ++it) {
          int e = tid + 512 * it;
          if (e < 1920) {
            int hh = e >= 960, e2 = e - 960 * hh;
            int st = e2 >= 768, e3 = e2 - 768 * st;
            int t = st ? e3 >> 6 : e3 >> 8;
            int a = st ? (e3 >> 1) & 31 : (e3 >> 3) & 31;
            int i = st ? e3 & 1 : e3 & 7;
            int gg = a >> 3, f = 9 * ((a >> 2) & 1) + 4 * t + (a & 3);
            int d = st ? (gg == 0 ? 16 + i : i) : (8 * (gg & 1) + 2 * (gg >> 1) + i);
            bool ok = (4 * t + (a & 3) < 9) && !(st && (gg & 1));
            int src = ((2 * hh + (gg >> 1)) * 18 + d) * 18 + f;
            unsigned short v = *(LAS unsigned short*)(L + L_KVT + (ok ? src : 0) * 2);
            *(LAS unsigned short*)(L + L_PT + e * 2) = ok ? v : (unsigned short)0;
          }
        }
      }
    }
    if (s == 3 && tid == 448) {
      __hip_atomic_store((GAS u64*)(P.ws + W_FO + ((br * 16 + rt) * 8 + hg) * 8), tag, __ATOMIC_RELAXED, __HIP_MEMORY_SCOPE_AGENT);
      if (ISA) __hip_atomic_store((GAS u64*)(P.ws + W_FK + (hg * 16 + rt) * 8), tag, __ATOMIC_RELAXED, __HIP_MEMORY_SCOPE_AGENT);
    }
    const int sb = slot_of(s);
    const int wa_s = opq(wa) + sb + 18432, wsd_s = opq(wsd) + sb + 18432, xa_s = opq(xa0) + sb;
    const int wh_s = wa_s + hoff, xh_s = xa_s + (128 - 32 * r) * 128;
#pragma unroll
    for (int ks = 0; ks < 2; ++ks) {
      const int kk = 2 * s + ks;
      xf[kk][0] = lds128(L, xa_s ^ (ks * 64));
      xf[kk][1] = lds128(L, (xa_s ^ (ks * 64)) + 2048);
      bf16x8 wf[5];
      wf[0] = trfrag<144>(L, ks * 4608 + wa_s);
      wf[1] = trfrag<144>(L, ks * 4608 + wa_s + 8);
      wf[2] = trfrag<144>(L, 9216 + ks * 4608 + wa_s);
      wf[3] = trfrag<144>(L, 9216 + ks * 4608 + wa_s + 8);
      wf[4] = trfrag<144>(L, ks * 4608 + wsd_s);
#pragma unroll
      for (int t = 0; t < 5; ++t) {
        acc[t][0] = mfma(wf[t], asb(xf[kk][0]), acc[t][0]);
        acc[t][1] = mfma(wf[t], asb(xf[kk][1]), acc[t][1]);
      }
      if (!ISA) {
        u32x4 xh = lds128(L, xh_s ^ (ks * 64));
        hacc[0] = mfma(trfrag<144>(L, ks * 4608 + wh_s), asb(xh), hacc[0]);
        hacc[1] = mfma(wf[4], asb(xh), hacc[1]);
      }
    }
  }

  const bool act = (c >> 3) == (g >> 1);
  const int pta = L_PT + h * 1920 + (g * 8 + (c & 7)) * 16;
  const int ptb = L_PT + h * 1920 + 1536 + (g * 8 + (c & 7)) * 4;

  u32x4 km[2], vm[2];
  unsigned ex[2];
  float exf[2][2];
#pragma unroll
  for (int mt = 0; mt < 2; ++mt) {
    km[mt] = u32x4{pk2(acc[0][mt][0], acc[0][mt][1]), pk2(acc[0][mt][2], acc[0][mt][3]), pk2(acc[1][mt][0], acc[1][mt][1]), pk2(acc[1][mt][2], acc[1][mt][3])};
    exf[mt][0] = g < 2 ? acc[4][mt][0] : acc[4][mt][2];
    exf[mt][1] = g < 2 ? acc[4][mt][1] : acc[4][mt][3];
    ex[mt] = pk2(exf[mt][0], exf[mt][1]);
  }
  u32x4 own[4]; u32x2 ownx;
  if (ISA) {
#pragma unroll
    for (int mt = 0; mt < 2; ++mt)
      vm[mt] = u32x4{pk2(acc[2][mt][0], acc[2][mt][1]), pk2(acc[2][mt][2], acc[2][mt][3]), pk2(acc[3][mt][0], acc[3][mt][1]), pk2(acc[3][mt][2], acc[3][mt][3])};
    unsigned ph[2][9];
    features(L, pta, ptb, act, km, ex, ph);
    bar();
    const int pc = g & 1;
#pragma unroll
    for (int mt = 0; mt < 2; ++mt) {
      const int rp = 32 * r + 16 * mt + c;
      const int base = L_REC + ((2 * h + (g >> 1)) * 8 + pc * 4) * 2048;
      const int r0 = rp * 16, r1 = (rp ^ 8) * 16;
      *(LAS u32x4*)(L + base + r0) = u32x4{ph[mt][0], ph[mt][1], ph[mt][2], ph[mt][3]};
      *(LAS u32x4*)(L + base + 2048 + r1) = u32x4{ph[mt][4], ph[mt][5], ph[mt][6], ph[mt][7]};
      *(LAS u32x4*)(L + base + 4096 + (pc ? r1 : r0)) = u32x4{ph[mt][8], vm[mt][0], vm[mt][1], vm[mt][2]};
      *(LAS u32x4*)(L + base + 6144 + (pc ? r1 : r0)) = u32x4{vm[mt][3], ex[mt], 0x00003F80u, 0u};
    }
    bar();
    {
      const int rowb = 16 * (g >> 1) + 4 * (g & 1) + tq;
      const int hb = L_REC + r * 8 * 2048 + (tp & 1) * 8;
      const int sel = tp >> 1;
      const int aM0 = hb + sel * 2048 + (rowb + 8 * sel) * 16;
      const int aM1 = aM0 + 4 * 2048;
      const int aM2 = hb + 2 * 2048 + sel * 4 * 2048 + (rowb + 8 * sel) * 16;
      const int aN = aM2 + h * 2048;
      f32x4 D0 = {0.f, 0.f, 0.f, 0.f}, D1 = D0, D2 = D0;
#pragma unroll
      for (int ks = 0; ks < 4; ++ks) {
        auto fr = [&](int a) {
          s16x4 u = __builtin_amdgcn_ds_read_tr16_b64_v4i16((LAS s16x4*)(L + a + ks * 512));
          s16x4 v = __builtin_amdgcn_ds_read_tr16_b64_v4i16((LAS s16x4*)(L + ((a + ks * 512) ^ 128)));
          u32x2 ua = __builtin_bit_cast(u32x2, u), ub = __builtin_bit_cast(u32x2, v);
          return asb(u32x4{ua.x, ua.y, ub.x, ub.y});
        };
        bf16x8 n = fr(aN);
        D0 = mfma(fr(aM0), n, D0);
        D1 = mfma(fr(aM1), n, D1);
        D2 = mfma(fr(aM2), n, D2);
      }
      unsigned a0 = pk2(D0[0], D0[1]), a1 = pk2(D0[2], D0[3]), b0 = pk2(D1[0], D1[1]), b1 = pk2(D1[2], D1[3]);
      swap16(a0, b0); swap16(a1, b1);
      unsigned t0 = pk2(D2[0], D2[1]), t1 = t0;
      swap32(t0, t1);
      store_kv(u32x4{canon(a0), canon(a1), canon(b0), canon(b1)}, u32x2{canon(t0), canon(t1)});
    }
  } else {
    own[0] = km[0]; own[1] = km[1];
    own[2] = u32x4{pk2(acc[2][0][0], acc[2][1][0]), pk2(acc[2][0][1], acc[2][1][1]), pk2(acc[2][0][2], acc[2][1][2]), pk2(acc[2][0][3], acc[2][1][3])};
    own[3] = u32x4{pk2(acc[3][0][0], acc[3][1][0]), pk2(acc[3][0][1], acc[3][1][1]), pk2(acc[3][0][2], acc[3][1][2]), pk2(acc[3][0][3], acc[3][1][3])};
    unsigned v0 = pk2(exf[0][0], exf[1][0]), v1 = pk2(exf[0][1], exf[1][1]);
    ownx = (g & 1) ? u32x2{v0, v1} : u32x2{ex[0], ex[1]};
    bar();
    const int rec = (4 * h + g) * 80 + 8 + 16 * r + c;
#pragma unroll
    for (int q = 0; q < 4; ++q) *(LAS u32x4*)(L + L_REC + q * 10240 + rec * 16) = own[q];
    *(LAS u32x2*)(L + L_REC + 40960 + rec * 8) = ownx;
    const int hrec = (4 * h + g) * 80 + 2 * (c >> 2) + (c & 1), lh = (c >> 1) & 1;
    unsigned h0 = pk2(hacc[0][0], hacc[0][1]), h1 = pk2(hacc[0][2], hacc[0][3]);
    if (r < 2) {
      *(LAS u32x2*)(L + L_REC + lh * 10240 + hrec * 16 + r * 8) = u32x2{h0, h1};
    } else {
      LAS unsigned short* d = (LAS unsigned short*)(L + L_REC + r * 10240 + hrec * 16 + lh * 2);
      d[0] = (unsigned short)h0; d[2] = (unsigned short)(h0 >> 16); d[4] = (unsigned short)h1; d[6] = (unsigned short)(h1 >> 16);
    }
    if (r == 0) {
      unsigned e = g < 2 ? pk2(hacc[1][0], hacc[1][1]) : pk2(hacc[1][2], hacc[1][3]);
      LAS unsigned short* d = (LAS unsigned short*)(L + L_REC + 40960 + hrec * 8);
      d[(g & 1) ? lh : 2 * lh] = (unsigned short)e;
      d[(g & 1) ? 2 + lh : 2 * lh + 1] = (unsigned short)(e >> 16);
    }
  }

  constexpr int NPUB = ISA ? 2 : 0;
  f32x4 qa[3][2];
#pragma unroll
  for (int t = 0; t < 3; ++t) { qa[t][0] = f32x4{0.f, 0.f, 0.f, 0.f}; qa[t][1] = qa[t][0]; }
  auto qstep = [&](int kk) {
    const int eb = (kk >> 1) * 9216 + (kk & 1) * 4608;
    bf16x8 w0 = trfrag<144>(L, eb + wa), w1 = trfrag<144>(L, eb + wa + 8), w2 = trfrag<144>(L, eb + wsd);
#pragma unroll
    for (int mt = 0; mt < 2; ++mt) {
      qa[0][mt] = mfma(w0, asb(xf[kk][mt]), qa[0][mt]);
      qa[1][mt] = mfma(w1, asb(xf[kk][mt]), qa[1][mt]);
      qa[2][mt] = mfma(w2, asb(xf[kk][mt]), qa[2][mt]);
    }
  };
  waitbar<6 + NPUB, 5 + NPUB>(wid);
#pragma unroll
  for (int kk = 0; kk < 8; ++kk) qstep(kk);
  waitv<1 + NPUB>();
  bar();
  qjobs(true);
  issue4(WO, L_S1);
#pragma unroll
  for (int kk = 8; kk < 16; ++kk) qstep(kk);
  if (wid == 7) {
    const GAS u64* fo = (const GAS u64*)(P.ws + W_FO + (br * 16 + rt) * 64);
    const GAS u64* fk = (const GAS u64*)(P.ws + W_FK + hg * 128);
    const GAS u64* fp = (lane < 8 || !ISA) ? fo + (lane & 7) : fk + (lane & 15);
    for (int spin = 0; spin < SPIN_CAP; ++spin) {
      u64 v = __hip_atomic_load(fp, __ATOMIC_RELAXED, __HIP_MEMORY_SCOPE_AGENT);
      if (__all(v == tag)) break;
      __builtin_amdgcn_s_sleep(2);
    }
  }
  waitbar<5, 4>(wid);
  issue4(WO + 288 * 1152, L_S2);
  qstep(16); qstep(17);
  bar();
  {
#pragma unroll
    for (int i = 0; i < 2; ++i) {
      int U = (wid + 8 * i) * 64 + lane;
      int R = U / 9, un = U - 9 * R;
      int k = kmap(R & 63);
      int row = 36 * (k >> 2) + 16 + (k & 3);
      if (i == 0) glds16(WO, row * 1152 + hg * 144 + un * 16, L_S3 + wid * 1024);
      else glds16_lt<1>(wid, WO, row * 1152 + hg * 144 + un * 16, L_S3 + 8192);
    }
  }
  u32x4 qm[2]; unsigned qx[2];
#pragma unroll
  for (int mt = 0; mt < 2; ++mt) {
    qm[mt] = u32x4{pk2(qa[0][mt][0], qa[0][mt][1]), pk2(qa[0][mt][2], qa[0][mt][3]), pk2(qa[1][mt][0], qa[1][mt][1]), pk2(qa[1][mt][2], qa[1][mt][3])};
    unsigned e = g < 2 ? pk2(qa[2][mt][0], qa[2][mt][1]) : pk2(qa[2][mt][2], qa[2][mt][3]);
    qx[mt] = (g & 1) ? 0u : e;
  }

  u32x4 om[2]; unsigned ox[2];
  if (ISA) {
    u32x4 part[16];
    const int kvl = W_KV + hg * 16 * 5760 + (tid < 360 ? tid : 0) * 16;
#pragma unroll
    for (int i = 0; i < 16; ++i) part[i] = __builtin_amdgcn_raw_buffer_load_b128(wsr, kvl + i * 5760, 0, 16);
    unsigned ph[2][9];
    features(L, pta, ptb, act, qm, qx, ph);
    for (int spin = 0; spin < SPIN_CAP; ++spin) {
      unsigned m = 0;
#pragma unroll
      for (int i = 0; i < 16; ++i) { m = umax3(m, part[i][0], part[i][1]); m = umax3(m, part[i][2], part[i][3]); }
      if (__all(m != SENT)) break;
      __builtin_amdgcn_s_sleep(1);
#pragma unroll
      for (int i = 0; i < 16; ++i) part[i] = __builtin_amdgcn_raw_buffer_load_b128(wsr, kvl + i * 5760, 0, 16);
    }
    {
      float s[8];
      const unsigned selLo = (unsigned)opq(0x00003F80), selHi = (unsigned)opq(0x3F800000);
#pragma unroll
      for (int j = 0; j < 8; ++j) s[j] = 0.f;
#pragma unroll
      for (int i = 0; i < 16; ++i) {
#pragma unroll
        for (int j = 0; j < 4; ++j) {
          s[2 * j] = dot2(part[i][j], selLo, s[2 * j]);
          s[2 * j + 1] = dot2(part[i][j], selHi, s[2 * j + 1]);
        }
      }
      if (tid < 360) *(LAS u32x4*)(L + L_KVT + tid * 16) = u32x4{pk2(s[0], s[1]), pk2(s[2], s[3]), pk2(s[4], s[5]), pk2(s[6], s[7])};
    }
    waitv<0>();
    bar();
    const int hb = L_KVT + (2 * h + (g >> 1)) * 1440;
    f32x4 O[2][3];
#pragma unroll
    for (int t = 0; t < 3; ++t) {
      const int d = t < 2 ? ((c >> 2) & 1) * 8 + 4 * t + (c & 3) : 16 + (c & 3);
      const int a0 = act ? hb + d * 64 + (g & 1) * 32 : L_ZERO;
      const int a2 = act ? hb + 1280 + d * 8 + (g & 1) * 4 : L_ZERO;
      u32x4 T0 = lds128(L, a0), T1 = lds128(L, a0 + 16);
      u32x4 T2 = {lds32(L, a2), 0u, 0u, 0u};
#pragma unroll
      for (int mt = 0; mt < 2; ++mt) {
        f32x4 z = {0.f, 0.f, 0.f, 0.f};
        z = mfma(asb(T0), asb(u32x4{ph[mt][0], ph[mt][1], ph[mt][2], ph[mt][3]}), z);
        z = mfma(asb(T1), asb(u32x4{ph[mt][4], ph[mt][5], ph[mt][6], ph[mt][7]}), z);
        O[mt][t] = mfma(asb(T2), asb(u32x4{ph[mt][8], 0u, 0u, 0u}), z);
      }
    }
#pragma unroll
    for (int mt = 0; mt < 2; ++mt) {
      float rd = __builtin_amdgcn_rcpf(O[mt][2][2] + 1e-6f);
      om[mt] = u32x4{pk2(O[mt][0][0] * rd, O[mt][0][1] * rd), pk2(O[mt][0][2] * rd, O[mt][0][3] * rd), pk2(O[mt][1][0] * rd, O[mt][1][1] * rd), pk2(O[mt][1][2] * rd, O[mt][1][3] * rd)};
      ox[mt] = pk2(O[mt][2][0] * rd, O[mt][2][1] * rd);
    }
  } else {
    const int rec = (4 * h + g) * 80 + 8 + 16 * r + c;
    u32x4 R[5][4]; u32x2 RX[5];
#pragma unroll
    for (int q = 0; q < 4; ++q) R[0][q] = own[q];
    RX[0] = ownx;
#pragma unroll
    for (int j = 1; j < 5; ++j) {
#pragma unroll
      for (int q = 0; q < 4; ++q) if (!(j == 4 && q == 0)) R[j][q] = lds128(L, L_REC + q * 10240 + (rec - 2 * j) * 16);
      RX[j] = lds64(L, L_REC + 40960 + (rec - 2 * j) * 8);
    }
    auto qk = [&](int mt, int j, int lh) {
      float a = dot2(qx[mt], RX[j][lh], 0.f);
#pragma unroll
      for (int d = 0; d < 4; ++d) a = dot2(qm[mt][d], R[j][lh][d], a);
      return a;
    };
    float lg[8];
#pragma unroll
    for (int sl = 0; sl < 8; ++sl) {
      float a = sl == 0 ? qk(0, 4, 1) : qk(0, sl >> 1, (sl & 1) ^ 1);
      float b = sl == 0 ? qk(1, 0, 1) : qk(1, sl >> 1, (sl & 1) ^ 1);
      swap16f(a, b);
      lg[sl] = (a + b) * 0.23570226039551584f;
    }
    if (rt == 0 && r == 0) {
      const int i = c >> 1;
#pragma unroll
      for (int sl = 0; sl < 8; ++sl) {
        int j = sl == 0 ? ((g & 1) ? 0 : 4) : (sl >> 1);
        lg[sl] = i < j ? -1e9f : lg[sl];
      }
    }
    float mx = lg[0];
#pragma unroll
    for (int sl = 1; sl < 8; ++sl) mx = __builtin_fmaxf(mx, lg[sl]);
    const float LOG2E = 1.4426950408889634f;
    float sum = 0.f;
#pragma unroll
    for (int sl = 0; sl < 8; ++sl) { lg[sl] = __builtin_amdgcn_exp2f((lg[sl] - mx) * LOG2E); sum += lg[sl]; }
    float rs = __builtin_amdgcn_rcpf(sum);
    unsigned ul[5], uh[4];
#pragma unroll
    for (int j = 0; j < 4; ++j) {
      unsigned a = pk2(lg[2 * j + 1] * rs, lg[2 * j] * rs), b = a;
      swap16(a, b);
      ul[j] = a; uh[j] = b;
    }
    ul[4] = ul[0] & 0xFFFF0000u; ul[0] &= 0x0000FFFFu;
    float ol[10], oh[10];
#pragma unroll
    for (int d = 0; d < 10; ++d) {
      float a = 0.f, b = 0.f;
#pragma unroll
      for (int j = 0; j < 5; ++j) {
        unsigned v = d < 8 ? R[j][2 + (d >> 2)][d & 3] : RX[j][d - 8];
        a = dot2(ul[j], v, a);
        if (j < 4) b = dot2(uh[j], v, b);
      }
      ol[d] = a; oh[d] = b;
    }
    om[0] = u32x4{pk2(ol[0], ol[1]), pk2(ol[2], ol[3]), pk2(ol[4], ol[5]), pk2(ol[6], ol[7])};
    om[1] = u32x4{pk2(oh[0], oh[1]), pk2(oh[2], oh[3]), pk2(oh[4], oh[5]), pk2(oh[6], oh[7])};
    ox[0] = pk2(ol[8], ol[9]); ox[1] = pk2(oh[8], oh[9]);
  }
#pragma unroll
  for (int mt = 0; mt < 2; ++mt) {
    unsigned t0 = ox[mt], t1 = t0;
    swap32(t0, t1);
    store_o(mt, u32x4{canon(om[mt][0]), canon(om[mt][1]), canon(om[mt][2]), canon(om[mt][3])}, u32x2{canon(t0), canon(t1)});
  }
  if (!ISA) { waitv<4>(); bar(); }

  {
    const int ob = W_OB + ((br * 16 + rt) * 8 + wid) * 18432;
    u32x2 rm[4];
    for (int spin = 0; spin < SPIN_CAP; ++spin) {
#pragma unroll
      for (int i = 0; i < 4; ++i) rm[i] = __builtin_amdgcn_raw_buffer_load_b64(wsr, ob + 16384 + ((i >> 1) * 8 + 2 * g + (i & 1)) * 128 + c * 8, 0, 16);
      unsigned m = 0;
#pragma unroll
      for (int i = 0; i < 4; ++i) m = umax3(m, rm[i][0], rm[i][1]);
      if (__all(m != SENT)) break;
      __builtin_amdgcn_s_sleep(1);
    }
    u32x4 of[18];
    for (int spin = 0; spin < SPIN_CAP; ++spin) {
#pragma unroll
      for (int i = 0; i < 16; ++i) of[i] = __builtin_amdgcn_raw_buffer_load_b128(wsr, ob + i * 1024 + lane * 16, 0, 16);
      unsigned m = 0;
#pragma unroll
      for (int i = 0; i < 16; ++i) { m = umax3(m, of[i][0], of[i][1]); m = umax3(m, of[i][2], of[i][3]); }
      if (__all(m != SENT)) break;
      __builtin_amdgcn_s_sleep(1);
    }
    of[16] = u32x4{rm[0][0], rm[0][1], rm[1][0], rm[1][1]};
    of[17] = u32x4{rm[2][0], rm[2][1], rm[3][0], rm[3][1]};
    const int oa = wrow + (tp == 0 ? 0 : tp == 1 ? 2 : tp == 2 ? 5 : 7) * 8;
    const int os = wrow + ((tp & 1) ? 13 : 4) * 8;
    f32x4 C[5];
#pragma unroll
    for (int t = 0; t < 5; ++t) C[t] = f32x4{0.f, 0.f, 0.f, 0.f};
#pragma unroll
    for (int kk = 0; kk < 18; ++kk) {
      const int eb = (kk >> 1) * 9216 + (kk & 1) * 4608;
      C[0] = mfma(trfrag<144>(L, eb + oa), asb(of[kk]), C[0]);
      C[1] = mfma(trfrag<144>(L, eb + oa + 8), asb(of[kk]), C[1]);
      C[2] = mfma(trfrag<144>(L, eb + oa + 72), asb(of[kk]), C[2]);
      C[3] = mfma(trfrag<144>(L, eb + oa + 80), asb(of[kk]), C[3]);
      C[4] = mfma(trfrag<144>(L, eb + os), asb(of[kk]), C[4]);
    }
    const int row = rt * 128 + 32 * (wid >> 1) + 4 * (c >> 1) + (c & 1) + 2 * (wid & 1);
    char* o = P.out + (size_t)row * 2304 + br * 1152 + hg * 144;
    const int cu = (g == 0 ? 0 : g == 1 ? 2 : g == 2 ? 5 : 7) * 8;
    u32x2* o0 = (u32x2*)(o + cu);
    o0[0] = u32x2{pk2(C[0][0], C[0][1]), pk2(C[0][2], C[0][3])};
    o0[1] = u32x2{pk2(C[1][0], C[1][1]), pk2(C[1][2], C[1][3])};
    o0[9] = u32x2{pk2(C[2][0], C[2][1]), pk2(C[2][2], C[2][3])};
    o0[10] = u32x2{pk2(C[3][0], C[3][1]), pk2(C[3][2], C[3][3])};
    if (g < 2) *(u32x2*)(o + (g ? 13 : 4) * 8) = u32x2{pk2(C[4][0], C[4][1]), pk2(C[4][2], C[4][3])};
  }
}

__global__ __launch_bounds__(512, 2) void fused(Params P) {
  extern __shared__ __attribute__((aligned(16))) char lds_raw[];
  LAS char* L = (LAS char*)lds_raw;
  const int b = blockIdx.x, xcd = b & 7, idx = b >> 3;
  const int br = xcd >> 2, xq = xcd & 3;
  const int rt = (xq >> 1) * 8 + (idx >> 2), hg = (xq & 1) * 4 + (idx & 3);
  const u64 E = *(const __attribute__((address_space(4))) u64*)(uintptr_t)(P.ws + W_EPOCH);
  const u64 tag = (E ^ 0x9E3779B97F4A7C15ull) * 0xD1342543DE82EF95ull + 0x632BE59BD9B4E019ull;
  if (br == 0) body<true>(P, L, rt, hg, tag);
  else body<false>(P, L, rt, hg, tag);
  if (b == 255 && threadIdx.x < 64) {
    const GAS u64* f = (const GAS u64*)(P.ws + W_FO) + threadIdx.x;
    bool ok = false;
    for (int spin = 0; spin < SPIN_CAP; ++spin) {
      bool e = true;
#pragma unroll
      for (int i = 0; i < 4; ++i) e &= __hip_atomic_load(f + 64 * i, __ATOMIC_RELAXED, __HIP_MEMORY_SCOPE_AGENT) == tag;
      if (__all(e)) { ok = true; break; }
      __builtin_amdgcn_s_sleep(2);
    }
    (void)ok;
    if (threadIdx.x == 0) __hip_atomic_store((GAS u64*)(P.ws + W_EPOCH), tag, __ATOMIC_RELAXED, __HIP_MEMORY_SCOPE_AGENT);
  }
}

extern "C" void kernel_launch(void* const* d_in, const int* in_sizes, int n_in, void* d_out, int out_size, void* d_ws,
                              size_t ws_size, hipStream_t stream) {
  Params P;
  P.x[0] = (const char*)d_in[0]; P.w[0] = (const char*)d_in[1]; P.wphi = (const char*)d_in[2]; P.wo[0] = (const char*)d_in[3];
  P.x[1] = (const char*)d_in[4]; P.w[1] = (const char*)d_in[5]; P.wo[1] = (const char*)d_in[6];
  P.out = (char*)d_out; P.ws = (char*)d_ws;
  (void)hipFuncSetAttribute((const void*)fused, hipFuncAttributeMaxDynamicSharedMemorySize, LDS_BYTES);
  hipLaunchKernelGGL(fused, dim3(256), dim3(512), LDS_BYTES, stream, P);
}
```

```cpp
#include <hip/hip_runtime.h>
#include <stdint.h>
#include <type_traits>

typedef short s16x4 __attribute__((ext_vector_type(4)));
typedef __bf16 bf16x8 __attribute__((ext_vector_type(8)));
typedef __bf16 bf16x2 __attribute__((ext_vector_type(2)));
typedef float f32x4 __attribute__((ext_vector_type(4)));
typedef float f32x2 __attribute__((ext_vector_type(2)));
typedef unsigned u32x4 __attribute__((ext_vector_type(4)));
typedef unsigned u32x2 __attribute__((ext_vector_type(2)));
typedef unsigned long long u64;
#define LAS __attribute__((address_space(3)))
#define GAS __attribute__((address_space(1)))
#define DEV __device__ __forceinline__
#define SB() __builtin_amdgcn_sched_barrier(0)

constexpr int L_K = 0;
constexpr int L_Q = 0;
constexpr int L_K2 = 46080;
constexpr int L_X = 92160;
constexpr int L_REC = L_X;
constexpr int L_ZERO = L_REC + 65536;
constexpr int L_PT = L_ZERO + 64;
constexpr int L_KVT = L_PT + 1920;
constexpr int L_END = L_KVT + 2880;
static_assert(L_END <= 162816, "lds");
constexpr int LDS_BYTES = 163840;

constexpr int W_EPOCH = 0;
constexpr int W_F = 4096;
constexpr int W_KV = 65536;
constexpr int W_HB = 524288;
constexpr int W_OB = 1048576;
constexpr int W_TOTAL = W_OB + 2 * 8 * 16 * 18432;
constexpr unsigned SENT = 0xFFFFFFFFu;
constexpr int OOB = 0x40000000;
constexpr int SPIN_CAP = 20000;

template <int N> DEV void waitv() { asm volatile("s_waitcnt vmcnt(%0)" ::"n"(N) : "memory"); }
DEV void bar() { asm volatile("s_waitcnt lgkmcnt(0)\n\ts_barrier" ::: "memory"); }
template <int N> DEV void waitbar() { asm volatile("s_waitcnt vmcnt(%0)\n\ts_barrier" ::"n"(N) : "memory"); }

DEV unsigned pk2(float a, float b) {
  f32x2 v = {a, b};
  bf16x2 r = __builtin_convertvector(v, bf16x2);
  return __builtin_bit_cast(unsigned, r);
}
DEV bf16x8 asb(u32x4 v) { return __builtin_bit_cast(bf16x8, v); }
DEV f32x4 mfma(bf16x8 a, bf16x8 b, f32x4 c) { return __builtin_amdgcn_mfma_f32_16x16x32_bf16(a, b, c, 0, 0, 0); }
DEV float dot2(unsigned a, unsigned b, float c) {
  return __builtin_amdgcn_fdot2_f32_bf16(__builtin_bit_cast(bf16x2, a), __builtin_bit_cast(bf16x2, b), c, false);
}
DEV u32x4 lds128(LAS char* L, int off) { return *(LAS u32x4*)(L + off); }
DEV u32x2 lds64(LAS char* L, int off) { return *(LAS u32x2*)(L + off); }
DEV unsigned lds32(LAS char* L, int off) { return *(LAS unsigned*)(L + off); }
DEV bf16x8 trfrag(LAS char* L, int off) {
  s16x4 a = __builtin_amdgcn_ds_read_tr16_b64_v4i16((LAS s16x4*)(L + off));
  s16x4 b = __builtin_amdgcn_ds_read_tr16_b64_v4i16((LAS s16x4*)(L + off + 1280));
  u32x2 ua = __builtin_bit_cast(u32x2, a), ub = __builtin_bit_cast(u32x2, b);
  u32x4 r = {ua.x, ua.y, ub.x, ub.y};
  return asb(r);
}
DEV void glds60(const char* base, unsigned voff, int dst) {
  asm volatile("s_mov_b32 m0, %2\n\ts_lshr_b64 exec, -1, 4\n\tglobal_load_lds_dwordx4 %0, %1\n\ts_mov_b64 exec, -1"
               ::"v"(voff), "s"(base), "s"(dst) : "memory", "m0", "scc");
}
DEV void glds64(const char* base, unsigned voff, int dst) {
  asm volatile("s_mov_b32 m0, %2\n\ts_nop 0\n\tglobal_load_lds_dwordx4 %0, %1" ::"v"(voff), "s"(base), "s"(dst) : "memory", "m0");
}
template <int OFF> DEV void gload128(u32x4& d, const char* base, unsigned voff) {
  asm volatile("global_load_dwordx4 %0, %1, %2 offset:%3" : "=v"(d) : "v"(voff), "s"(base), "n"(OFF) : "memory");
}
#define STAMP(i) do {} while (0)
#define RSTAMP(i) do {} while (0)
DEV int opq(int v) { asm volatile("" : "+v"(v)); return v; }
DEV void swap16(unsigned& a, unsigned& b) {
  auto r = __builtin_amdgcn_permlane16_swap(a, b, false, false);
  a = r[0]; b = r[1];
}
DEV void swap32(unsigned& a, unsigned& b) {
  auto r = __builtin_amdgcn_permlane32_swap(a, b, false, false);
  a = r[0]; b = r[1];
}
DEV void swap16f(float& a, float& b) {
  unsigned x = __float_as_uint(a), y = __float_as_uint(b);
  swap16(x, y);
  a = __uint_as_float(x); b = __uint_as_float(y);
}
DEV unsigned canon(unsigned x) { return x < 0xFFFFFFFEu ? x : 0xFFFFFFFEu; }
DEV unsigned umax3(unsigned a, unsigned b, unsigned c) { unsigned m = a > b ? a : b; return m > c ? m : c; }
DEV u32x4 canon4(u32x4 v) { return u32x4{canon(v[0]), canon(v[1]), canon(v[2]), canon(v[3])}; }

struct Params {
  const char* x[2];
  const char* w[2];
  const char* wo[2];
  const char* wphi;
  char* out;
  char* ws;
};

DEV int kmap(int p) { return (p & ~12) | ((p & 4) << 1) | ((p & 8) >> 1); }
DEV int worow(int k) {
  int m = k + 4 * (((k >> 4) + 1) >> 1);
  int kr = k - 512;
  int r = 36 * (kr >> 2) + 16 + (kr & 3);
  return k < 512 ? m : r;
}

enum { OP_W = 0, OP_X = 100, OP_Q = 200, OP_FLAG = 300, OP_NOP = 400 };
constexpr int NLOOP = 64;
constexpr int XS(int j, int i) { return OP_X + 4 * j + i; }
constexpr int LOOP_OPS[NLOOP] = {
    XS(2, 0), XS(2, 1), XS(2, 2), XS(2, 3), OP_W + 4, OP_W + 5, OP_NOP, OP_NOP,
    XS(3, 0), XS(3, 1), XS(3, 2), XS(3, 3), OP_W + 6, OP_W + 7, OP_NOP, OP_NOP,
    XS(4, 0), XS(4, 1), XS(4, 2), XS(4, 3), OP_FLAG, OP_W + 8, OP_W + 9, OP_NOP,
    XS(5, 0), XS(5, 1), XS(5, 2), XS(5, 3), OP_W + 10, OP_W + 11, OP_NOP, OP_NOP,
    XS(6, 0), XS(6, 1), XS(6, 2), XS(6, 3), OP_NOP, OP_NOP, OP_NOP, OP_NOP,
    XS(7, 0), XS(7, 1), XS(7, 2), XS(7, 3), OP_Q + 0, OP_Q + 1, OP_Q + 2, OP_NOP,
    XS(8, 0), XS(8, 1), XS(8, 2), XS(8, 3), OP_Q + 3, OP_Q + 4, OP_Q + 5, OP_NOP,
    OP_NOP, OP_NOP, OP_NOP, OP_NOP, OP_NOP, OP_NOP, OP_NOP, OP_NOP};
constexpr int NPRO = 15;
constexpr int issued_at(int k, int nph) {
  int n = NPRO + nph;
  for (int i = 0; i < 4 * k && i < NLOOP; ++i) n += LOOP_OPS[i] != OP_NOP;
  return n;
}
constexpr int op_pos(int code, int nph) {
  if (code == OP_W + 0) return 1;
  if (code == OP_W + 1) return 2;
  if (code >= OP_X && code < OP_X + 4) return 3 + code - OP_X;
  if (code >= OP_X + 4 && code < OP_X + 8) return 7 + nph + code - OP_X - 4;
  if (code == OP_W + 2) return 11 + nph;
  if (code == OP_W + 3) return 12 + nph;
  int n = NPRO + nph;
  for (int i = 0; i < NLOOP; ++i) { n += LOOP_OPS[i] != OP_NOP; if (LOOP_OPS[i] == code) return n; }
  return -1000;
}

DEV void features(LAS char* L, int pta, int ptb, bool act, const u32x4 (&m)[2], const unsigned (&e)[2], unsigned (&ph)[2][9]) {
  f32x4 P[2][3];
#pragma unroll
  for (int t = 0; t < 3; ++t) {
    u32x4 T0 = lds128(L, act ? pta + t * 512 : L_ZERO);
    u32x4 T1 = {lds32(L, act ? ptb + t * 128 : L_ZERO), 0u, 0u, 0u};
#pragma unroll
    for (int mt = 0; mt < 2; ++mt) {
      f32x4 z = {0.f, 0.f, 0.f, 0.f};
      u32x4 E = {e[mt], 0u, 0u, 0u};
      z = mfma(asb(T0), asb(m[mt]), z);
      P[mt][t] = mfma(asb(T1), asb(E), z);
    }
  }
  const float LOG2E = 1.4426950408889634f;
  float p[2][9], mx[2], mn[2];
#pragma unroll
  for (int mt = 0; mt < 2; ++mt) {
#pragma unroll
    for (int i = 0; i < 9; ++i) p[mt][i] = P[mt][i >> 2][i & 3];
    float a = p[mt][0], b = p[mt][0];
#pragma unroll
    for (int i = 1; i < 9; ++i) { a = __builtin_fmaxf(a, p[mt][i]); b = __builtin_fminf(b, p[mt][i]); }
    float a2 = a, b2 = b;
    swap16f(a, a2); swap16f(b, b2);
    mx[mt] = __builtin_fmaxf(a, a2) * LOG2E; mn[mt] = __builtin_fminf(b, b2) * LOG2E;
  }
#pragma unroll
  for (int mt = 0; mt < 2; ++mt) {
    float ep[9], en[9], sp = 0.f, sn = 0.f;
#pragma unroll
    for (int i = 0; i < 9; ++i) {
      ep[i] = __builtin_amdgcn_exp2f(__builtin_fmaf(p[mt][i], LOG2E, -mx[mt]));
      en[i] = __builtin_amdgcn_exp2f(__builtin_fmaf(p[mt][i], -LOG2E, mn[mt]));
      sp += ep[i]; sn += en[i];
    }
    float sp2 = sp, sn2 = sn;
    swap16f(sp, sp2); swap16f(sn, sn2);
    float rp = __builtin_amdgcn_rcpf(sp + sp2), rn = __builtin_amdgcn_rcpf(sn + sn2);
    float f[18];
#pragma unroll
    for (int i = 0; i < 9; ++i) { f[i] = ep[i] * rp; f[9 + i] = en[i] * rn; }
#pragma unroll
    for (int i = 0; i < 9; ++i) ph[mt][i] = pk2(f[2 * i], f[2 * i + 1]);
  }
}

template <int N, class F> DEV void sfor(F&& f) {
  if constexpr (N > 0) { sfor<N - 1>(f); f(std::integral_constant<int, N - 1>{}); }
}

template <bool ISA>
DEV void body(const char* X, const char* W, const char* WO, const char* PHI, char* OUT, char* WS, LAS char* L, int rt, int hp, u64& tag) {
  const int br = ISA ? 0 : 1;
  constexpr int NPH = ISA ? 2 : 0;
  const int tid = threadIdx.x;
  const int wid = __builtin_amdgcn_readfirstlane(tid >> 6);
  const int lane = tid & 63;
  const int g = lane >> 4, c = lane & 15;
  RSTAMP(30); STAMP(0);

  const int r6 = (lane * 26) >> 8, un = lane - 10 * r6;
  const int kap = kmap(6 * wid + r6);
  const int part = un >= 5, u5 = un - 5 * part;
  const int wcol = ((hp * 72) & ~15) + u5 * 16 + kap * 3456;
  const unsigned wkv = (unsigned)(wcol + (1 + part) * 1152);
  const int r8 = lane >> 3;
  const unsigned xo0 = (unsigned)((32 * wid + 4 * (r8 >> 1) + (r8 & 1)) * 1152 + (((lane & 7) ^ (r8 >> 1)) * 16));
  const unsigned xo1 = (xo0 ^ 64u) + 16 * 1152;
  u32x4 xf[18][2];
  const int dW = wid * 960, dX = L_X + wid * 4096;
  auto opW = [&](int rho) { glds60(W + rho * (48 * 3456), wkv, L_K + rho * 7680 + dW); };
  auto opX = [&](int I) {
    const int j = I >> 2, i = I & 3;
    glds64(X + j * 128 + (i >> 1) * 2304, (i & 1) ? xo1 : xo0, dX + (j & 1) * 32768 + i * 1024);
  };
  opW(0); opW(1);
  opX(0); opX(1); opX(2); opX(3);
  SB();
  __amdgpu_buffer_rsrc_t wsr = __builtin_amdgcn_make_buffer_rsrc(WS, 0, W_TOTAL, 0x00020000);
  unsigned phv = 0, phw = 0;
  if (ISA) {
    __amdgpu_buffer_rsrc_t pr = __builtin_amdgcn_make_buffer_rsrc((void*)(PHI + hp * 1296), 0, 1296, 0x00020000);
    int st = tid >= 384, e3 = tid - 384 * st;
    int t = st ? e3 >> 5 : e3 >> 7;
    int a = st ? e3 & 31 : (e3 >> 2) & 31;
    int gg = a >> 3, f = 9 * ((a >> 2) & 1) + 4 * t + (a & 3);
    int d = st ? (gg == 0 ? 16 : 0) : (8 * (gg & 1) + 2 * (gg >> 1) + 2 * (e3 & 3));
    bool ok = (4 * t + (a & 3) < 9) && !(st && (gg & 1)) && tid < 480;
    int src = ok ? (((gg >> 1) * 18 + d) * 18 + f) * 2 : OOB;
    asm volatile("buffer_load_ushort %0, %2, %3, 0 offen\n\tbuffer_load_ushort %1, %2, %3, 0 offen offset:36"
                 : "=&v"(phv), "=&v"(phw) : "v"(src), "s"(pr) : "memory");
  }
  asm volatile("s_barrier" ::: "memory");
  opX(4); opX(5); opX(6); opX(7);
  opW(2); opW(3);

  const int obt = W_OB + (br * 8 + rt) * 16 * 18432;
  const int obm = obt + 2 * wid * 18432 + hp * 1024 + lane * 16;
  const int xb = ISA ? W_KV + (hp * 8 + rt) * 2880 : W_HB + (rt * 16 + hp) * 2304;
  {
    u32x4 s4 = {SENT, SENT, SENT, SENT};
    int u = tid - 128;
    int m1 = opq(obt + (tid >> 3) * 18432 + 16384 + hp * 128 + (tid & 7) * 16), m2 = opq(xb + u * 16);
    int mo = tid < 128 ? m1 : (u < (ISA ? 180 : 144) ? m2 : OOB);
    __builtin_amdgcn_raw_buffer_store_b128(s4, wsr, obm, 0, 16);
    __builtin_amdgcn_raw_buffer_store_b128(s4, wsr, obm + 18432, 0, 16);
    __builtin_amdgcn_raw_buffer_store_b128(s4, wsr, mo, 0, 16);
  }
  if (tid < 16) *(LAS unsigned*)(L + L_ZERO + tid * 4) = 0u;

  const int tq = (lane >> 2) & 3, tp = lane & 3;
  const int wrow = (16 * (g >> 1) + 4 * (g & 1) + tq) * 160;
  const int unit = 16 * (tp & 1) + 40 * (tp >> 1);
  const int wa = wrow + unit + (hp & 1) * 8;
  const int wsd = wrow + 32 + (hp & 1) * 8 + (tp & 1) * 80;
  const unsigned wq = (unsigned)(wcol + part * (288 * 3456));
  const int flo = tid == 448 ? W_F + ((br * 8 + rt) * 16 + hp) * 8 : OOB;

  f32x4 acc[5][2];
#pragma unroll
  for (int t = 0; t < 5; ++t) { acc[t][0] = f32x4{0.f, 0.f, 0.f, 0.f}; acc[t][1] = acc[t][0]; }

  auto loop_op = [&](auto J) {
    if constexpr (J < NLOOP) {
      constexpr int code = LOOP_OPS[J];
      if constexpr (code < OP_X) opW(code);
      else if constexpr (code < OP_Q) opX(code - OP_X);
      else if constexpr (code < OP_FLAG) glds60(W + (code - OP_Q) * (48 * 3456), wq, L_Q + (code - OP_Q) * 7680 + dW);
      else if constexpr (code == OP_FLAG) __builtin_amdgcn_raw_buffer_store_b64(u32x2{(unsigned)tag, (unsigned)(tag >> 32)}, wsr, flo, 0, 16);
    }
  };

  STAMP(1);
  asm volatile("s_waitcnt lgkmcnt(0)" : "+s"(tag) :: "memory");
  tag = (tag ^ 0x9E3779B97F4A7C15ull) * 0xD1342543DE82EF95ull + 0x632BE59BD9B4E019ull;
  waitbar<issued_at(0, NPH) - op_pos(XS(0, 3), NPH)>();
  STAMP(2);
  bf16x8 wf[2][5];
  int wa_s = opq(wa + L_K), wsd_s = opq(wsd + L_K);
  const int xa0 = opq(dX + c * 128 + ((g ^ (c >> 1)) * 16)), xa1 = opq(xa0 ^ 64);
  xf[0][0] = lds128(L, xa0); xf[0][1] = lds128(L, xa0 + 2048);
  wf[0][0] = trfrag(L, wa_s); wf[0][1] = trfrag(L, wa_s + 8); wf[0][2] = trfrag(L, wa_s + 80); wf[0][3] = trfrag(L, wa_s + 88);
  xf[1][0] = lds128(L, xa1); xf[1][1] = lds128(L, xa1 + 2048);
  wf[0][4] = trfrag(L, wsd_s);
  sfor<18>([&](auto K) {
    constexpr int k = K, cur = k & 1, nxt = cur ^ 1;
    constexpr int kn = k + 1, no = (kn % 12) * 5120;
    constexpr bool rdx = (k & 1) && kn < 18;
    constexpr int xs = ((kn >> 1) & 1) * 32768;
    constexpr bool wb = kn % 3 == 0 && kn < 18;
    constexpr int nX = rdx ? issued_at(k, NPH) - op_pos(XS(kn >> 1, 3), NPH) : 63;
    constexpr int nW = wb ? issued_at(k, NPH) - op_pos(OP_W + 2 * (kn / 3) + 1, NPH) : 63;
    if constexpr (wb) waitbar<(nX < nW ? nX : nW)>();
    else if constexpr (rdx) waitv<nX>();
    if constexpr ((k & 1) == 0)
      asm volatile("" : "+v"(xf[k][0]), "+v"(xf[k][1]), "+v"(xf[k + 1][0]), "+v"(xf[k + 1][1]) :: "memory");
    if constexpr (ISA && k == 1) {
      asm volatile("" : "+v"(phv), "+v"(phw));
      if (tid < 480) *(LAS unsigned*)(L + L_PT + tid * 4) = phv | (phw << 16);
    }
    if constexpr (kn == 12) { wa_s = opq(wa + L_K + 12 * 5120); wsd_s = opq(wsd + L_K + 12 * 5120); }
    SB();
    acc[0][0] = mfma(wf[cur][0], asb(xf[k][0]), acc[0][0]);
    if constexpr (rdx) { xf[kn][0] = lds128(L, xa0 + xs); xf[kn][1] = lds128(L, xa0 + xs + 2048); }
    if constexpr (kn < 18) wf[nxt][0] = trfrag(L, wa_s + no);
    SB();
    acc[0][1] = mfma(wf[cur][0], asb(xf[k][1]), acc[0][1]);
    loop_op(std::integral_constant<int, 4 * k>{});
    SB();
    acc[1][0] = mfma(wf[cur][1], asb(xf[k][0]), acc[1][0]);
    if constexpr (kn < 18) wf[nxt][1] = trfrag(L, wa_s + no + 8);
    SB();
    acc[1][1] = mfma(wf[cur][1], asb(xf[k][1]), acc[1][1]);
    loop_op(std::integral_constant<int, 4 * k + 1>{});
    SB();
    acc[2][0] = mfma(wf[cur][2], asb(xf[k][0]), acc[2][0]);
    if constexpr (kn < 18) wf[nxt][2] = trfrag(L, wa_s + no + 80);
    if constexpr (rdx) { xf[kn + 1][0] = lds128(L, xa1 + xs); xf[kn + 1][1] = lds128(L, xa1 + xs + 2048); }
    SB();
    acc[2][1] = mfma(wf[cur][2], asb(xf[k][1]), acc[2][1]);
    loop_op(std::integral_constant<int, 4 * k + 2>{});
    SB();
    acc[3][0] = mfma(wf[cur][3], asb(xf[k][0]), acc[3][0]);
    if constexpr (kn < 18) wf[nxt][3] = trfrag(L, wa_s + no + 88);
    SB();
    acc[3][1] = mfma(wf[cur][3], asb(xf[k][1]), acc[3][1]);
    loop_op(std::integral_constant<int, 4 * k + 3>{});
    SB();
    acc[4][0] = mfma(wf[cur][4], asb(xf[k][0]), acc[4][0]);
    if constexpr (kn < 18) wf[nxt][4] = trfrag(L, wsd_s + no);
    SB();
    acc[4][1] = mfma(wf[cur][4], asb(xf[k][1]), acc[4][1]);
    SB();
    if constexpr (k == 5 || k == 11) STAMP(3 + (k == 11));
  });
  STAMP(5);

  const bool act = (c >> 3) == (g >> 1);
  const int pta = L_PT + (g * 8 + (c & 7)) * 16;
  const int ptb = L_PT + 1536 + (g * 8 + (c & 7)) * 4;
  const int cg = hp >> 1;
  const int wocol = cg * 144 + (un > 8 ? 8 : un) * 16;
  auto opWO = [&](int rho, unsigned off, int rowbase) {
    glds60(WO + rowbase * 1152, off, (rho < 6 ? L_K2 + rho * 7680 : L_Q + (rho - 6) * 7680) + dW);
  };

  u32x4 km[2], vm[2];
  unsigned ex[2];
  float exf[2][2];
#pragma unroll
  for (int mt = 0; mt < 2; ++mt) {
    km[mt] = u32x4{pk2(acc[0][mt][0], acc[0][mt][1]), pk2(acc[0][mt][2], acc[0][mt][3]), pk2(acc[1][mt][0], acc[1][mt][1]), pk2(acc[1][mt][2], acc[1][mt][3])};
    exf[mt][0] = g < 2 ? acc[4][mt][0] : acc[4][mt][2];
    exf[mt][1] = g < 2 ? acc[4][mt][1] : acc[4][mt][3];
    ex[mt] = pk2(exf[mt][0], exf[mt][1]);
  }
  u32x4 own[4]; u32x2 ownx;
  const int rec = g * 144 + 8 + 16 * wid + c;
  if (ISA) {
#pragma unroll
    for (int mt = 0; mt < 2; ++mt)
      vm[mt] = u32x4{pk2(acc[2][mt][0], acc[2][mt][1]), pk2(acc[2][mt][2], acc[2][mt][3]), pk2(acc[3][mt][0], acc[3][mt][1]), pk2(acc[3][mt][2], acc[3][mt][3])};
    unsigned ph[2][9];
    features(L, pta, ptb, act, km, ex, ph);
    STAMP(6);
    waitv<0>();
    bar();
    STAMP(7);
    const int pc = g & 1;
#pragma unroll
    for (int mt = 0; mt < 2; ++mt) {
      const int rp = 32 * wid + 16 * mt + c;
      const int base = L_REC + g * 4 * 4096;
      const int r0 = rp * 16, r1 = (rp ^ 8) * 16;
      *(LAS u32x4*)(L + base + r0) = u32x4{ph[mt][0], ph[mt][1], ph[mt][2], ph[mt][3]};
      *(LAS u32x4*)(L + base + 4096 + r1) = u32x4{ph[mt][4], ph[mt][5], ph[mt][6], ph[mt][7]};
      *(LAS u32x4*)(L + base + 8192 + (pc ? r1 : r0)) = u32x4{ph[mt][8], vm[mt][0], vm[mt][1], vm[mt][2]};
      *(LAS u32x4*)(L + base + 12288 + (pc ? r1 : r0)) = u32x4{vm[mt][3], ex[mt], 0x00003F80u, 0u};
    }
    bar();
    STAMP(8);
    {
      const int hd = wid & 1, nt = (wid >> 1) & 1;
      const int rowb = 16 * (g >> 1) + 4 * (g & 1) + tq;
      const int hb = L_REC + hd * 8 * 4096 + (tp & 1) * 8;
      const int sel = tp >> 1;
      const int aM0 = hb + sel * 4096 + (rowb + 8 * sel) * 16;
      const int aM2 = hb + 2 * 4096 + sel * 4 * 4096 + (rowb + 8 * sel) * 16;
      const int aN = aM2 + nt * 4096;
      int a = (c & 7) >= 2 ? (c >> 3) * 8 + (c & 7) - 2 : -1;
      int b = c < 2 ? 6 + c : (c >= 8 && c < 14) ? 6 + c : -1;
      const int dI = nt == 0 ? a : b;
      auto fr = [&](int ad, int ks) {
        s16x4 u = __builtin_amdgcn_ds_read_tr16_b64_v4i16((LAS s16x4*)(L + ad + ks * 512));
        s16x4 v = __builtin_amdgcn_ds_read_tr16_b64_v4i16((LAS s16x4*)(L + (ad ^ 128) + ks * 512));
        u32x2 ua = __builtin_bit_cast(u32x2, u), ub = __builtin_bit_cast(u32x2, v);
        return asb(u32x4{ua.x, ua.y, ub.x, ub.y});
      };
      f32x4 D0 = {0.f, 0.f, 0.f, 0.f}, D1 = D0;
      if (wid < 4) {
#pragma unroll
        for (int ks = 0; ks < 8; ++ks) {
          bf16x8 n = fr(aN, ks);
          D0 = mfma(fr(aM0, ks), n, D0);
          D1 = mfma(fr(aM0 + 4 * 4096, ks), n, D1);
        }
        unsigned a0 = pk2(D0[0], D0[1]), a1 = pk2(D0[2], D0[3]), b0 = pk2(D1[0], D1[1]), b1 = pk2(D1[2], D1[3]);
        swap16(a0, b0); swap16(a1, b1);
        const int kvm = dI >= 0 ? xb + hd * 1440 + dI * 64 + (((g & 1) << 1) | (g >> 1)) * 16 : OOB;
        __builtin_amdgcn_raw_buffer_store_b128(canon4(u32x4{a0, a1, b0, b1}), wsr, kvm, 0, 16);
      } else {
#pragma unroll
        for (int ks = 0; ks < 8; ks += 2) {
          D0 = mfma(fr(aM2, ks), fr(aN, ks), D0);
          D1 = mfma(fr(aM2, ks + 1), fr(aN, ks + 1), D1);
        }
        unsigned t0 = pk2(D0[0] + D1[0], D0[1] + D1[1]), t1 = t0;
        swap32(t0, t1);
        const int kvx = (dI >= 0 && g == 0) ? xb + hd * 1440 + 1280 + dI * 8 : OOB;
        __builtin_amdgcn_raw_buffer_store_b64(u32x2{canon(t0), canon(t1)}, wsr, kvx, 0, 16);
      }
    }
  } else {
    own[0] = km[0]; own[1] = km[1];
    own[2] = u32x4{pk2(acc[2][0][0], acc[2][1][0]), pk2(acc[2][0][1], acc[2][1][1]), pk2(acc[2][0][2], acc[2][1][2]), pk2(acc[2][0][3], acc[2][1][3])};
    own[3] = u32x4{pk2(acc[3][0][0], acc[3][1][0]), pk2(acc[3][0][1], acc[3][1][1]), pk2(acc[3][0][2], acc[3][1][2]), pk2(acc[3][0][3], acc[3][1][3])};
    unsigned v0 = pk2(exf[0][0], exf[1][0]), v1 = pk2(exf[0][1], exf[1][1]);
    ownx = (g & 1) ? u32x2{v0, v1} : u32x2{ex[0], ex[1]};
    waitv<0>();
    if (wid == 7) {
      const int ho = c >= 8 ? xb + (g * 8 + c - 8) * 16 : OOB;
#pragma unroll
      for (int q = 0; q < 4; ++q) __builtin_amdgcn_raw_buffer_store_b128(canon4(own[q]), wsr, ho + q * 512, 0, 16);
      __builtin_amdgcn_raw_buffer_store_b64(u32x2{canon(ownx[0]), canon(ownx[1])}, wsr, c >= 8 ? xb + 2048 + (g * 8 + c - 8) * 8 : OOB, 0, 16);
    }
    STAMP(6);
    bar();
    STAMP(7);
#pragma unroll
    for (int q = 0; q < 4; ++q) *(LAS u32x4*)(L + L_REC + q * 9216 + rec * 16) = own[q];
    *(LAS u32x2*)(L + L_REC + 36864 + rec * 8) = ownx;
  }

  STAMP(9);
  f32x4 qa[3][2];
#pragma unroll
  for (int t = 0; t < 3; ++t) { qa[t][0] = f32x4{0.f, 0.f, 0.f, 0.f}; qa[t][1] = qa[t][0]; }
  u32x2 flg;
  {
    const unsigned woE = (unsigned)(worow(kap) * 1152 + wocol), woO = (unsigned)(worow(48 + kap) * 1152 + wocol);
    const unsigned fof = (unsigned)(W_F + br * 1024 + (lane < 16 ? rt * 16 + lane : (lane & 7) * 16 + hp) * 8);
    const int wq_s = opq(wa + L_Q), wsq_s = opq(wsd + L_Q);
    wf[0][0] = trfrag(L, wq_s); wf[0][1] = trfrag(L, wq_s + 8); wf[0][2] = trfrag(L, wsq_s);
    sfor<18>([&](auto K) {
      constexpr int k = K, cur = k & 1, nxt = cur ^ 1;
      constexpr int kn = k + 1, no = (kn % 9) * 5120 + (kn / 9) * 80;
      SB();
      qa[0][0] = mfma(wf[cur][0], asb(xf[k][0]), qa[0][0]);
      if constexpr (kn < 18) wf[nxt][0] = trfrag(L, wq_s + no);
      SB();
      qa[0][1] = mfma(wf[cur][0], asb(xf[k][1]), qa[0][1]);
      if constexpr (kn < 18) wf[nxt][1] = trfrag(L, wq_s + no + 8);
      SB();
      qa[1][0] = mfma(wf[cur][1], asb(xf[k][0]), qa[1][0]);
      if constexpr (kn < 18) wf[nxt][2] = trfrag(L, wsq_s + no);
      SB();
      qa[1][1] = mfma(wf[cur][1], asb(xf[k][1]), qa[1][1]);
      if constexpr (k < 6) opWO(k, (k & 1) ? woO : woE, 108 * (k >> 1));
      if constexpr (k == 12) asm volatile("global_load_dwordx2 %0, %1, %2 sc1" : "=v"(flg) : "v"(fof), "s"(WS) : "memory");
      SB();
      qa[2][0] = mfma(wf[cur][2], asb(xf[k][0]), qa[2][0]);
      qa[2][1] = mfma(wf[cur][2], asb(xf[k][1]), qa[2][1]);
      SB();
    });
    STAMP(10);
    bar();
    STAMP(11);
    const unsigned wo10 = (unsigned)(worow(480 + kap) * 1152 + wocol), wo11 = (unsigned)(worow(528 + kap) * 1152 + wocol);
    opWO(6, woE, 324); opWO(7, woO, 324); opWO(8, woE, 432); opWO(9, woO, 432); opWO(10, wo10, 0); opWO(11, wo11, 0);
    asm volatile("s_waitcnt vmcnt(6)" : "+v"(flg) :: "memory");
    if (!__all(flg[0] == (unsigned)tag && flg[1] == (unsigned)(tag >> 32))) {
      const GAS u64* fp = (const GAS u64*)(WS + fof);
      for (int spin = 0; spin < SPIN_CAP; ++spin) {
        u64 v = __hip_atomic_load(fp, __ATOMIC_RELAXED, __HIP_MEMORY_SCOPE_AGENT);
        if (__all(v == tag)) break;
        __builtin_amdgcn_s_sleep(2);
      }
    }
  }
  STAMP(12);
  u32x4 qm[2]; unsigned qx[2];
#pragma unroll
  for (int mt = 0; mt < 2; ++mt) {
    qm[mt] = u32x4{pk2(qa[0][mt][0], qa[0][mt][1]), pk2(qa[0][mt][2], qa[0][mt][3]), pk2(qa[1][mt][0], qa[1][mt][1]), pk2(qa[1][mt][2], qa[1][mt][3])};
    unsigned e = g < 2 ? pk2(qa[2][mt][0], qa[2][mt][1]) : pk2(qa[2][mt][2], qa[2][mt][3]);
    qx[mt] = (g & 1) ? 0u : e;
  }

  u32x4 om[2]; unsigned ox[2];
  if (ISA) {
    u32x4 part4[4];
    const int kvl = tid < 360 ? W_KV + (hp * 8 + (tid & 1) * 4) * 2880 + (tid >> 1) * 16 : OOB;
#pragma unroll
    for (int i = 0; i < 4; ++i) part4[i] = __builtin_amdgcn_raw_buffer_load_b128(wsr, kvl + i * 2880, 0, 16);
    unsigned ph[2][9];
    features(L, pta, ptb, act, qm, qx, ph);
    STAMP(13);
    for (int spin = 0; spin < SPIN_CAP; ++spin) {
      unsigned m = 0;
#pragma unroll
      for (int i = 0; i < 4; ++i) { m = umax3(m, part4[i][0], part4[i][1]); m = umax3(m, part4[i][2], part4[i][3]); }
      if (__all(m != SENT)) break;
      __builtin_amdgcn_s_sleep(1);
#pragma unroll
      for (int i = 0; i < 4; ++i) part4[i] = __builtin_amdgcn_raw_buffer_load_b128(wsr, kvl + i * 2880, 0, 16);
    }
    STAMP(14);
    {
      float s[8];
      const unsigned selLo = (unsigned)opq(0x00003F80), selHi = (unsigned)opq(0x3F800000);
#pragma unroll
      for (int j = 0; j < 8; ++j) s[j] = 0.f;
#pragma unroll
      for (int i = 0; i < 4; ++i) {
#pragma unroll
        for (int j = 0; j < 4; ++j) {
          s[2 * j] = dot2(part4[i][j], selLo, s[2 * j]);
          s[2 * j + 1] = dot2(part4[i][j], selHi, s[2 * j + 1]);
        }
      }
#pragma unroll
      for (int j = 0; j < 8; ++j)
        s[j] += __uint_as_float(__builtin_amdgcn_update_dpp(0u, __float_as_uint(s[j]), 0xB1, 0xF, 0xF, true));
      if (tid < 360 && !(tid & 1)) *(LAS u32x4*)(L + L_KVT + (tid >> 1) * 16) = u32x4{pk2(s[0], s[1]), pk2(s[2], s[3]), pk2(s[4], s[5]), pk2(s[6], s[7])};
    }
    waitv<0>();
    bar();
    STAMP(15);
    const int hb = L_KVT + (g >> 1) * 1440;
    f32x4 O[2][3];
#pragma unroll
    for (int t = 0; t < 3; ++t) {
      const int d = t < 2 ? ((c >> 2) & 1) * 8 + 4 * t + (c & 3) : 16 + (c & 3);
      const int a0 = act ? hb + d * 64 + (g & 1) * 32 : L_ZERO;
      const int a2 = act ? hb + 1280 + d * 8 + (g & 1) * 4 : L_ZERO;
      u32x4 T0 = lds128(L, a0), T1 = lds128(L, a0 + 16);
      u32x4 T2 = {lds32(L, a2), 0u, 0u, 0u};
#pragma unroll
      for (int mt = 0; mt < 2; ++mt) {
        f32x4 z = {0.f, 0.f, 0.f, 0.f};
        z = mfma(asb(T0), asb(u32x4{ph[mt][0], ph[mt][1], ph[mt][2], ph[mt][3]}), z);
        z = mfma(asb(T1), asb(u32x4{ph[mt][4], ph[mt][5], ph[mt][6], ph[mt][7]}), z);
        O[mt][t] = mfma(asb(T2), asb(u32x4{ph[mt][8], 0u, 0u, 0u}), z);
      }
    }
#pragma unroll
    for (int mt = 0; mt < 2; ++mt) {
      float rd = __builtin_amdgcn_rcpf(O[mt][2][2] + 1e-6f);
      om[mt] = u32x4{pk2(O[mt][0][0] * rd, O[mt][0][1] * rd), pk2(O[mt][0][2] * rd, O[mt][0][3] * rd), pk2(O[mt][1][0] * rd, O[mt][1][1] * rd), pk2(O[mt][1][2] * rd, O[mt][1][3] * rd)};
      ox[mt] = pk2(O[mt][2][0] * rd, O[mt][2][1] * rd);
    }
  } else {
    if (wid == 0) {
      u32x4 hq[4]; u32x2 hx;
      if (rt > 0) {
        const int hs = W_HB + ((rt - 1) * 16 + hp) * 2304, hl = g * 8 + (c & 7);
        for (int spin = 0; spin < SPIN_CAP; ++spin) {
          unsigned m = 0;
#pragma unroll
          for (int q = 0; q < 4; ++q) hq[q] = __builtin_amdgcn_raw_buffer_load_b128(wsr, hs + q * 512 + hl * 16, 0, 16);
          hx = __builtin_amdgcn_raw_buffer_load_b64(wsr, hs + 2048 + hl * 8, 0, 16);
#pragma unroll
          for (int q = 0; q < 4; ++q) { m = umax3(m, hq[q][0], hq[q][1]); m = umax3(m, hq[q][2], hq[q][3]); }
          m = umax3(m, hx[0], hx[1]);
          if (__all(m != SENT)) break;
          __builtin_amdgcn_s_sleep(1);
        }
      } else {
        const int r0 = g * 144 + 8;
        hq[0] = lds128(L, L_REC + r0 * 16); hq[1] = hq[0];
        u32x4 a = lds128(L, L_REC + 2 * 9216 + r0 * 16), b = lds128(L, L_REC + 3 * 9216 + r0 * 16);
        u32x2 e = lds64(L, L_REC + 36864 + r0 * 8);
#pragma unroll
        for (int j = 0; j < 4; ++j) { hq[2][j] = (a[j] & 0xFFFFu) * 0x10001u; hq[3][j] = (b[j] & 0xFFFFu) * 0x10001u; }
        hx = (g & 1) ? u32x2{(e[0] & 0xFFFFu) * 0x10001u, (e[1] & 0xFFFFu) * 0x10001u} : u32x2{e[0], e[0]};
      }
      const int hr = g * 144 + (c & 7);
#pragma unroll
      for (int q = 0; q < 4; ++q) *(LAS u32x4*)(L + L_REC + q * 9216 + hr * 16) = hq[q];
      *(LAS u32x2*)(L + L_REC + 36864 + hr * 8) = hx;
    }
    STAMP(13);
    u32x4 R[5][4]; u32x2 RX[5];
#pragma unroll
    for (int q = 0; q < 4; ++q) R[0][q] = own[q];
    RX[0] = ownx;
#pragma unroll
    for (int j = 1; j < 5; ++j) {
#pragma unroll
      for (int q = 0; q < 4; ++q) if (!(j == 4 && q == 0)) R[j][q] = lds128(L, L_REC + q * 9216 + (rec - 2 * j) * 16);
      RX[j] = lds64(L, L_REC + 36864 + (rec - 2 * j) * 8);
    }
    auto qk = [&](int mt, int j, int lh) {
      float a = dot2(qx[mt], RX[j][lh], 0.f);
#pragma unroll
      for (int d = 0; d < 4; ++d) a = dot2(qm[mt][d], R[j][lh][d], a);
      return a;
    };
    float lg[8];
#pragma unroll
    for (int sl = 0; sl < 8; ++sl) {
      float a = sl == 0 ? qk(0, 4, 1) : qk(0, sl >> 1, (sl & 1) ^ 1);
      float b = sl == 0 ? qk(1, 0, 1) : qk(1, sl >> 1, (sl & 1) ^ 1);
      swap16f(a, b);
      lg[sl] = (a + b) * 0.23570226039551584f;
    }
    if (rt == 0 && wid == 0) {
      const int i = c >> 1;
#pragma unroll
      for (int sl = 0; sl < 8; ++sl) {
        int j = sl == 0 ? ((g & 1) ? 0 : 4) : (sl >> 1);
        lg[sl] = i < j ? -1e9f : lg[sl];
      }
    }
    float mx = lg[0];
#pragma unroll
    for (int sl = 1; sl < 8; ++sl) mx = __builtin_fmaxf(mx, lg[sl]);
    const float LOG2E = 1.4426950408889634f;
    float sum = 0.f;
#pragma unroll
    for (int sl = 0; sl < 8; ++sl) { lg[sl] = __builtin_amdgcn_exp2f((lg[sl] - mx) * LOG2E); sum += lg[sl]; }
    float rs = __builtin_amdgcn_rcpf(sum);
    unsigned ul[5], uh[4];
#pragma unroll
    for (int j = 0; j < 4; ++j) {
      unsigned a = pk2(lg[2 * j + 1] * rs, lg[2 * j] * rs), b = a;
      swap16(a, b);
      ul[j] = a; uh[j] = b;
    }
    ul[4] = ul[0] & 0xFFFF0000u; ul[0] &= 0x0000FFFFu;
    float ol[10], oh[10];
#pragma unroll
    for (int d = 0; d < 10; ++d) {
      float a = 0.f, b = 0.f;
#pragma unroll
      for (int j = 0; j < 5; ++j) {
        unsigned v = d < 8 ? R[j][2 + (d >> 2)][d & 3] : RX[j][d - 8];
        a = dot2(ul[j], v, a);
        if (j < 4) b = dot2(uh[j], v, b);
      }
      ol[d] = a; oh[d] = b;
    }
    om[0] = u32x4{pk2(ol[0], ol[1]), pk2(ol[2], ol[3]), pk2(ol[4], ol[5]), pk2(ol[6], ol[7])};
    om[1] = u32x4{pk2(oh[0], oh[1]), pk2(oh[2], oh[3]), pk2(oh[4], oh[5]), pk2(oh[6], oh[7])};
    ox[0] = pk2(ol[8], ol[9]); ox[1] = pk2(oh[8], oh[9]);
  }
  STAMP(16);
  {
    const int obr = g == 1 ? obt + 2 * wid * 18432 + 16384 + hp * 128 + c * 8 : OOB;
#pragma unroll
    for (int mt = 0; mt < 2; ++mt) {
      unsigned t0 = ox[mt], t1 = t0;
      swap32(t0, t1);
      __builtin_amdgcn_raw_buffer_store_b128(canon4(om[mt]), wsr, obm + mt * 18432, 0, 16);
      __builtin_amdgcn_raw_buffer_store_b64(u32x2{canon(t0), canon(t1)}, wsr, obr + mt * 18432, 0, 16);
    }
  }
  if (!ISA) { waitv<4>(); bar(); }
  STAMP(17);

  {
    const int tile = (hp & 1) * 8 + wid;
    const int ob = obt + tile * 18432;
    u32x4 of[18];
    unsigned pend = 0x3FFFFu;
    const int oa_m = ob + lane * 16, oa_r = ob + 16384 + 2 * g * 128 + c * 8;
    for (int spin = 0; spin < SPIN_CAP; ++spin) {
#pragma unroll
      for (int i = 0; i < 2; ++i)
        if (pend >> (16 + i) & 1) {
          u32x2 a = __builtin_amdgcn_raw_buffer_load_b64(wsr, oa_r + i * 1024, 0, 16);
          u32x2 b = __builtin_amdgcn_raw_buffer_load_b64(wsr, oa_r + i * 1024 + 128, 0, 16);
          of[16 + i] = u32x4{a[0], a[1], b[0], b[1]};
        }
#pragma unroll
      for (int i = 0; i < 16; ++i)
        if (pend >> i & 1) of[i] = __builtin_amdgcn_raw_buffer_load_b128(wsr, oa_m + i * 1024, 0, 16);
      unsigned np = 0;
#pragma unroll
      for (int i = 0; i < 18; ++i) {
        unsigned m = umax3(of[i][0], of[i][1], of[i][2]);
        m = m > of[i][3] ? m : of[i][3];
        np |= __any(m == SENT) ? 1u << i : 0u;
      }
      pend = np;
      if (!pend) break;
      __builtin_amdgcn_s_sleep(1);
    }
    STAMP(18);
    const int oa = wrow + unit;
    const int os = wrow + ((tp & 1) ? 13 : 4) * 8;
    f32x4 C[5];
#pragma unroll
    for (int t = 0; t < 5; ++t) C[t] = f32x4{0.f, 0.f, 0.f, 0.f};
    int oa_s = opq(oa + L_K2), os_s = opq(os + L_K2);
    wf[0][0] = trfrag(L, oa_s); wf[0][1] = trfrag(L, oa_s + 8); wf[0][2] = trfrag(L, oa_s + 72); wf[0][3] = trfrag(L, oa_s + 80);
    wf[0][4] = trfrag(L, os_s);
    sfor<18>([&](auto K) {
      constexpr int k = K, cur = k & 1, nxt = cur ^ 1;
      constexpr int kn = k + 1, no = (kn % 9) * 5120;
      if constexpr (kn == 9) { oa_s = opq(oa + L_Q); os_s = opq(os + L_Q); }
      SB();
      C[0] = mfma(wf[cur][0], asb(of[k]), C[0]);
      if constexpr (kn < 18) wf[nxt][0] = trfrag(L, oa_s + no);
      SB();
      C[1] = mfma(wf[cur][1], asb(of[k]), C[1]);
      if constexpr (kn < 18) wf[nxt][1] = trfrag(L, oa_s + no + 8);
      SB();
      C[2] = mfma(wf[cur][2], asb(of[k]), C[2]);
      if constexpr (kn < 18) wf[nxt][2] = trfrag(L, oa_s + no + 72);
      SB();
      C[3] = mfma(wf[cur][3], asb(of[k]), C[3]);
      if constexpr (kn < 18) wf[nxt][3] = trfrag(L, oa_s + no + 80);
      SB();
      C[4] = mfma(wf[cur][4], asb(of[k]), C[4]);
      if constexpr (kn < 18) wf[nxt][4] = trfrag(L, os_s + no);
      SB();
    });
    STAMP(19);
    const int row = rt * 256 + 32 * (tile >> 1) + 4 * (c >> 1) + (c & 1) + 2 * (tile & 1);
    char* o = OUT + (size_t)row * 2304 + br * 1152 + cg * 144;
    const int cu = 16 * (g & 1) + 40 * (g >> 1);
    u32x2* o0 = (u32x2*)(o + cu);
    o0[0] = u32x2{pk2(C[0][0], C[0][1]), pk2(C[0][2], C[0][3])};
    o0[1] = u32x2{pk2(C[1][0], C[1][1]), pk2(C[1][2], C[1][3])};
    o0[9] = u32x2{pk2(C[2][0], C[2][1]), pk2(C[2][2], C[2][3])};
    o0[10] = u32x2{pk2(C[3][0], C[3][1]), pk2(C[3][2], C[3][3])};
    if (g < 2) *(u32x2*)(o + (g ? 13 : 4) * 8) = u32x2{pk2(C[4][0], C[4][1]), pk2(C[4][2], C[4][3])};
  }
}

__global__ __launch_bounds__(512, 2) void fused(Params P) {
  extern __shared__ __attribute__((aligned(16))) char lds_raw[];
  LAS char* L = (LAS char*)lds_raw;
  const int b = blockIdx.x, xcd = b & 7, idx = b >> 3;
  const int br = xcd >> 2, xq = xcd & 3;
  const int rt = (xq >> 1) * 4 + (idx >> 3), hp = (xq & 1) * 8 + (idx & 7);
  const char* X = (br ? P.x[1] : P.x[0]) + (size_t)rt * (256 * 1152);
  const char* W = br ? P.w[1] : P.w[0];
  const char* WO = br ? P.wo[1] : P.wo[0];
  const char* PHI = P.wphi;
  char* OUT = P.out;
  char* WS = P.ws;
  asm volatile("" : "+s"(X), "+s"(W), "+s"(WO), "+s"(PHI), "+s"(OUT), "+s"(WS));
  u64 tag;
  asm volatile("s_load_dwordx2 %0, %1, 0x0" : "=s"(tag) : "s"(WS) : "memory");
  if (br == 0) body<true>(X, W, WO, PHI, OUT, WS, L, rt, hp, tag);
  else body<false>(X, W, WO, PHI, OUT, WS, L, rt, hp, tag);
  if (b == 255 && threadIdx.x < 64) {
    const GAS u64* f = (const GAS u64*)(WS + W_F) + threadIdx.x;
    for (int spin = 0; spin < SPIN_CAP; ++spin) {
      bool e = true;
#pragma unroll
      for (int i = 0; i < 4; ++i) e &= __hip_atomic_load(f + 64 * i, __ATOMIC_RELAXED, __HIP_MEMORY_SCOPE_AGENT) == tag;
      if (__all(e)) break;
      __builtin_amdgcn_s_sleep(2);
    }
    if (threadIdx.x == 0) __hip_atomic_store((GAS u64*)(WS + W_EPOCH), tag, __ATOMIC_RELAXED, __HIP_MEMORY_SCOPE_AGENT);
  }
}

extern "C" void kernel_launch(void* const* d_in, const int* in_sizes, int n_in, void* d_out, int out_size, void* d_ws,
                              size_t ws_size, hipStream_t stream) {
  Params P;
  P.x[0] = (const char*)d_in[0]; P.w[0] = (const char*)d_in[1]; P.wphi = (const char*)d_in[2]; P.wo[0] = (const char*)d_in[3];
  P.x[1] = (const char*)d_in[4]; P.w[1] = (const char*)d_in[5]; P.wo[1] = (const char*)d_in[6];
  P.out = (char*)d_out; P.ws = (char*)d_ws;
  (void)hipFuncSetAttribute((const void*)fused, hipFuncAttributeMaxDynamicSharedMemorySize, LDS_BYTES);
  hipLaunchKernelGGL(fused, dim3(256), dim3(512), LDS_BYTES, stream, P);
}
```

```cpp
#include <hip/hip_runtime.h>
#include <stdint.h>
#include <type_traits>
#pragma clang diagnostic ignored "-Winline-asm"

typedef short s16x4 __attribute__((ext_vector_type(4)));
typedef __bf16 bf16x8 __attribute__((ext_vector_type(8)));
typedef __bf16 bf16x2 __attribute__((ext_vector_type(2)));
typedef float f32x4 __attribute__((ext_vector_type(4)));
typedef float f32x2 __attribute__((ext_vector_type(2)));
typedef unsigned u32x4 __attribute__((ext_vector_type(4)));
typedef unsigned u32x2 __attribute__((ext_vector_type(2)));
typedef unsigned long long u64;
#define LAS __attribute__((address_space(3)))
#define GAS __attribute__((address_space(1)))
#define DEV __device__ __forceinline__
#define SB() __builtin_amdgcn_sched_barrier(0)

constexpr int SLOT = 48128;
constexpr int L_WO = 0;
constexpr int L_REC = 92160;
constexpr int L_ZERO = L_REC + 65536;
constexpr int L_PT = L_ZERO + 64;
constexpr int L_KVT = L_PT + 1920;
constexpr int L_DUMMY = L_KVT + 2880;
constexpr int L_END = L_DUMMY + 256;
static_assert(L_END <= 162816 && 3 * SLOT <= L_ZERO, "lds");
constexpr int LDS_BYTES = 163840;

constexpr int W_EPOCH = 0;
constexpr int W_F = 4096;
constexpr int W_KV = 65536;
constexpr int W_HB = 524288;
constexpr int W_OB = 1048576;
constexpr int W_TOTAL = W_OB + 2 * 8 * 16 * 18432;
constexpr unsigned SENT = 0xFFFFFFFFu;
constexpr int OOB = 0x40000000;
constexpr int SPIN_CAP = 400000;
#define KVSLEEP 24
#define LDSLEEP 8
#define BLDSLEEP 50
#define BTH 0
#define BTN 4
#define NPRO 14
#define R2SLEEP 20
#define NRA 2
#define PFD 4
#define J3K 0
#define J3S 1
#define KW 3
#define LEAD0 3
#define LEADN 2
#define LEAD 4
#define GSLEEP 2

template <int N> DEV void waitv() { asm volatile("s_waitcnt vmcnt(%0)" ::"n"(N) : "memory"); }
DEV void bar() { asm volatile("s_waitcnt lgkmcnt(0)\n\ts_barrier" ::: "memory"); }
template <int N> DEV void waitbar() { asm volatile("s_waitcnt vmcnt(%0)\n\ts_barrier" ::"n"(N) : "memory"); }

DEV unsigned pk2(float a, float b) {
  f32x2 v = {a, b};
  bf16x2 r = __builtin_convertvector(v, bf16x2);
  return __builtin_bit_cast(unsigned, r);
}
DEV bf16x8 asb(u32x4 v) { return __builtin_bit_cast(bf16x8, v); }
DEV f32x4 mfma(bf16x8 a, bf16x8 b, f32x4 c) { return __builtin_amdgcn_mfma_f32_16x16x32_bf16(a, b, c, 0, 0, 0); }
DEV float dot2(unsigned a, unsigned b, float c) {
  return __builtin_amdgcn_fdot2_f32_bf16(__builtin_bit_cast(bf16x2, a), __builtin_bit_cast(bf16x2, b), c, false);
}
DEV u32x4 lds128(LAS char* L, int off) { return *(LAS u32x4*)(L + off); }
DEV u32x2 lds64(LAS char* L, int off) { return *(LAS u32x2*)(L + off); }
DEV unsigned lds32(LAS char* L, int off) { return *(LAS unsigned*)(L + off); }
DEV bf16x8 trfrag(LAS char* L, int off) {
  s16x4 a = __builtin_amdgcn_ds_read_tr16_b64_v4i16((LAS s16x4*)(L + off));
  s16x4 b = __builtin_amdgcn_ds_read_tr16_b64_v4i16((LAS s16x4*)(L + off + 1280));
  u32x2 ua = __builtin_bit_cast(u32x2, a), ub = __builtin_bit_cast(u32x2, b);
  u32x4 r = {ua.x, ua.y, ub.x, ub.y};
  return asb(r);
}
DEV void glds60(const char* base, unsigned voff, int dst) {
  asm volatile("s_mov_b32 m0, %2\n\ts_lshr_b64 exec, -1, 4\n\tglobal_load_lds_dwordx4 %0, %1\n\ts_mov_b64 exec, -1"
               ::"v"(voff), "s"(base), "s"(dst) : "memory", "m0", "scc");
}
DEV void glds64(const char* base, unsigned voff, int dst) {
  asm volatile("s_mov_b32 m0, %2\n\ts_nop 0\n\tglobal_load_lds_dwordx4 %0, %1" ::"v"(voff), "s"(base), "s"(dst) : "memory", "m0");
}
template <int OFF> DEV void gload128(u32x4& d, const char* base, unsigned voff) {
  asm volatile("global_load_dwordx4 %0, %1, %2 offset:%3" : "=v"(d) : "v"(voff), "s"(base), "n"(OFF) : "memory");
}
DEV void gload64s(u32x2& d, const char* base, unsigned voff) {
  asm volatile("global_load_dwordx2 %0, %1, %2 sc1" : "=v"(d) : "v"(voff), "s"(base) : "memory");
}
#define STAMP(i) do {} while (0)
#define RSTAMP(i) do {} while (0)
DEV int opq(int v) { asm volatile("" : "+v"(v)); return v; }
DEV void swap16(unsigned& a, unsigned& b) {
  auto r = __builtin_amdgcn_permlane16_swap(a, b, false, false);
  a = r[0]; b = r[1];
}
DEV void swap32(unsigned& a, unsigned& b) {
  auto r = __builtin_amdgcn_permlane32_swap(a, b, false, false);
  a = r[0]; b = r[1];
}
DEV void swap16f(float& a, float& b) {
  unsigned x = __float_as_uint(a), y = __float_as_uint(b);
  swap16(x, y);
  a = __uint_as_float(x); b = __uint_as_float(y);
}
DEV unsigned canon(unsigned x) { return x < 0xFFFFFFFEu ? x : 0xFFFFFFFEu; }
DEV unsigned umax3(unsigned a, unsigned b, unsigned c) { unsigned m = a > b ? a : b; return m > c ? m : c; }
DEV u32x4 canon4(u32x4 v) { return u32x4{canon(v[0]), canon(v[1]), canon(v[2]), canon(v[3])}; }

struct Params {
  const char* x[2];
  const char* w[2];
  const char* wo[2];
  const char* wphi;
  char* out;
  char* ws;
};

DEV int kmap(int p) { return (p & ~12) | ((p & 4) << 1) | ((p & 8) >> 1); }
DEV int worow(int k) {
  int m = k + 4 * (((k >> 4) + 1) >> 1);
  int kr = k - 512;
  int r = 36 * (kr >> 2) + 16 + (kr & 3);
  return k < 512 ? m : r;
}

template <class F>
DEV void features(LAS char* L, int pta, int ptb, bool act, const u32x4 (&m)[2], const unsigned (&e)[2], unsigned (&ph)[2][9], F&& done) {
  f32x4 P[2][3];
#pragma unroll
  for (int t = 0; t < 3; ++t) {
    u32x4 T0 = lds128(L, act ? pta + t * 512 : L_ZERO);
    u32x4 T1 = {lds32(L, act ? ptb + t * 128 : L_ZERO), 0u, 0u, 0u};
#pragma unroll
    for (int mt = 0; mt < 2; ++mt) {
      f32x4 z = {0.f, 0.f, 0.f, 0.f};
      u32x4 E = {e[mt], 0u, 0u, 0u};
      z = mfma(asb(T0), asb(m[mt]), z);
      P[mt][t] = mfma(asb(T1), asb(E), z);
    }
  }
  const float LOG2E = 1.4426950408889634f;
  float p[2][9], mx[2], mn[2];
#pragma unroll
  for (int mt = 0; mt < 2; ++mt) {
#pragma unroll
    for (int i = 0; i < 9; ++i) p[mt][i] = P[mt][i >> 2][i & 3];
    float a = p[mt][0], b = p[mt][0];
#pragma unroll
    for (int i = 1; i < 9; ++i) { a = __builtin_fmaxf(a, p[mt][i]); b = __builtin_fminf(b, p[mt][i]); }
    float a2 = a, b2 = b;
    swap16f(a, a2); swap16f(b, b2);
    mx[mt] = __builtin_fmaxf(a, a2) * LOG2E; mn[mt] = __builtin_fminf(b, b2) * LOG2E;
  }
#pragma unroll
  for (int mt = 0; mt < 2; ++mt) {
    float ep[9], en[9], sp = 0.f, sn = 0.f;
#pragma unroll
    for (int i = 0; i < 9; ++i) {
      ep[i] = __builtin_amdgcn_exp2f(__builtin_fmaf(p[mt][i], LOG2E, -mx[mt]));
      en[i] = __builtin_amdgcn_exp2f(__builtin_fmaf(p[mt][i], -LOG2E, mn[mt]));
      sp += ep[i]; sn += en[i];
    }
    float sp2 = sp, sn2 = sn;
    swap16f(sp, sp2); swap16f(sn, sn2);
    float rp = __builtin_amdgcn_rcpf(sp + sp2), rn = __builtin_amdgcn_rcpf(sn + sn2);
    float f[18];
#pragma unroll
    for (int i = 0; i < 9; ++i) { f[i] = ep[i] * rp; f[9 + i] = en[i] * rn; }
#pragma unroll
    for (int i = 0; i < 9; ++i) ph[mt][i] = pk2(f[2 * i], f[2 * i + 1]);
    done(mt);
  }
}

template <int N, class F> DEV void sfor(F&& f) {
  if constexpr (N > 0) { sfor<N - 1>(f); f(std::integral_constant<int, N - 1>{}); }
}

DEV u64 next_tag(u64 t);
DEV u64 read_tag(const char* ws) {
  u64 t;
  asm volatile("s_load_dwordx2 %0, %1, 0x0\n\ts_waitcnt lgkmcnt(0)" : "=s"(t) : "s"(ws) : "memory");
  return next_tag(t);
}
DEV u64 next_tag(u64 t) { return (t ^ 0x9E3779B97F4A7C15ull) * 0xD1342543DE82EF95ull + 0x632BE59BD9B4E019ull; }

template <bool ISA>
DEV void loader(const char* XB, const char* W, const char* WO, char* WS, LAS char* L, int xcd, int jx, int wid) {
  const int rt = (xcd >> 1) * 2 + (jx >> 3), hp = (xcd & 1) * 8 + (jx & 7);
  const int lw = wid - 8;
  const char* X = XB + (size_t)(rt * 256 + lw * 64) * 1152;
  const int lane = threadIdx.x & 63;
  RSTAMP(30); STAMP(0);
  __builtin_amdgcn_s_setprio(2);
  __amdgpu_buffer_rsrc_t wsr = __builtin_amdgcn_make_buffer_rsrc(WS, 0, W_TOTAL, 0x00020000);
  u64 tag = 0;
  if (ISA) { if (LDSLEEP) __builtin_amdgcn_s_sleep(LDSLEEP); }
  else {
    const u32x4 s4 = {SENT, SENT, SENT, SENT};
    const int ob = W_OB + rt * 16 * 18432, u = lw * 64 + lane;
#pragma unroll
    for (int i = 0; i < 8; ++i)
      __builtin_amdgcn_raw_buffer_store_b128(s4, wsr, ob + lw * 4 * 18432 + hp * 1024 + lane * 16, (i & 3) * 18432 + (i >> 2) * (8 * 16 * 18432), 16);
    __builtin_amdgcn_raw_buffer_store_b128(s4, wsr, ob + (u >> 7) * (8 * 16 * 18432) + ((u >> 3) & 15) * 18432 + 16384 + hp * 128 + (u & 7) * 16, 0, 16);
    __builtin_amdgcn_raw_buffer_store_b128(s4, wsr, u < 180 ? W_KV + (hp * 8 + rt) * 2880 + u * 16 : OOB, 0, 16);
    __builtin_amdgcn_raw_buffer_store_b128(s4, wsr, u < 144 ? W_HB + (rt * 16 + hp) * 2304 + u * 16 : OOB, 0, 16);
    STAMP(21);
    tag = read_tag(WS);
    if (BLDSLEEP) __builtin_amdgcn_s_sleep(BLDSLEEP);
  }
  const int r8 = lane >> 3;
  const unsigned xo0 = (unsigned)((4 * (r8 >> 1) + (r8 & 1)) * 1152 + (((lane & 7) ^ (r8 >> 1)) * 16));
  const unsigned xo1 = (xo0 ^ 64u) + 16 * 1152;
  const int dW = lw * 3840, dX = 15360 + lw * 8192;
  auto opX = [&](int s, int i) {
    glds64(X + s * 128 + ((i >> 1) & 1) * 2304 + (i >> 2) * (32 * 1152), (i & 1) ? xo1 : xo0, (s % 3) * SLOT + dX + i * 1024);
  };
#pragma unroll
  for (int i = 0; i < 4; ++i) opX(0, i);
  SB();
  const int r6 = (lane * 26) >> 8, un = lane - 10 * r6;
  const int part = un >= 5, u5 = un - 5 * part;
  const int wcol = ((hp * 72) & ~15) + u5 * 16;
  int kap[4];
  unsigned wj[4];
#pragma unroll
  for (int j = 0; j < 4; ++j) {
    kap[j] = kmap(24 * lw + 6 * j + r6);
    wj[j] = (unsigned)(kap[j] < 64 ? wcol + (1 + part) * 1152 + kap[j] * 3456 : wcol + (kap[j] - 64 + 32 * part) * 3456);
  }
  auto opW = [&](int s, int j) { glds60(W + s * (64 * 3456), wj[j], (s % 3) * SLOT + dW + j * 960); };
#pragma unroll
  for (int j = 0; j < 4; ++j) opW(0, j);
#pragma unroll
  for (int i = 4; i < 8; ++i) opX(0, i);
  const int rto = xcd, cg = jx & 7;
  const int wocol = cg * 144 + (un > 8 ? 8 : un) * 16;
  unsigned wo[4], wl[4];
  const unsigned fo = (unsigned)(W_F + 1024 + (lane < 16 ? rto * 16 + lane : (lane & 7) * 16 + hp) * 8);
  u32x2 fv = {0u, 0u};
  sfor<10>([&](auto S) {
    constexpr int s = S + 1;
    if constexpr (s == 3) {
#pragma unroll
      for (int j = 0; j < 4; ++j) { wo[j] = (unsigned)(worow(kap[j]) * 1152 + wocol); wl[j] = (unsigned)(worow(480 + kap[j]) * 1152 + wocol); }
    }
    if constexpr (!ISA && s == 9) gload64s(fv, WS, fo);
    sfor<12>([&](auto I) {
      constexpr int i = I;
      if constexpr (s < 9) {
        if constexpr (i % 3 < 2) opX(s, 2 * (i / 3) + i % 3); else opW(s, i / 3);
      } else {
        constexpr int bk = 3 * (s - 9) + i / 4, j = i % 4;
        glds60(WO + (bk < 5 ? 108 * bk * 1152 : 0), bk < 5 ? wo[j] : wl[j], L_WO + bk * 15360 + dW + j * 960);
      }
      if constexpr (!ISA && BTH > 0 && s <= BTN && i % 3 == 2) __builtin_amdgcn_s_sleep(BTH);
      constexpr int ld = s <= LEADN ? LEAD0 : LEAD;
      if constexpr (i == ld - 1 && s < 10) {
        if constexpr (s < 6) { STAMP(s == 1 ? 1 : s == 2 ? 4 : s == 3 ? 7 : s == 4 ? 9 : 10); }
        waitbar<ld>();
        if constexpr (s < 5) { STAMP(s == 1 ? 2 : 9 + s); }
      }
    });
  });
  STAMP(5);
  asm volatile("s_barrier" : "+v"(fv) :: "memory");
  if (ISA && lw == 3) {
    tag = read_tag(WS);
    __builtin_amdgcn_raw_buffer_store_b64(u32x2{(unsigned)tag, (unsigned)(tag >> 32)}, wsr, lane == 0 ? W_F + (rt * 16 + hp) * 8 : OOB, 0, 16);
  }
  if (lw == 3 && (ISA || !__all(fv[0] == (unsigned)tag && fv[1] == (unsigned)(tag >> 32)))) {
    const GAS u64* fp = (const GAS u64*)(WS + fo);
#pragma unroll 1
    for (int spin = 0; spin < SPIN_CAP; ++spin) {
      u64 v = __hip_atomic_load(fp, __ATOMIC_RELAXED, __HIP_MEMORY_SCOPE_AGENT);
      if (__all(v == tag)) break;
      __builtin_amdgcn_s_sleep(2);
    }
  }
  STAMP(6);
  asm volatile("s_barrier" ::: "memory");
  STAMP(8);
  if (ISA && lw < 3) {
    const int u = lw * 64 + lane;
    const int kvl = u < 180 ? W_KV + hp * 8 * 2880 + u * 16 : OOB;
    u32x4 pt[8];
    unsigned pend = 0xFFu;
    __builtin_amdgcn_s_sleep(KVSLEEP);
#pragma unroll 1
    for (int spin = 0; spin < SPIN_CAP; ++spin) {
#pragma unroll
      for (int i = 0; i < 8; ++i)
        if (pend >> i & 1) pt[i] = __builtin_amdgcn_raw_buffer_load_b128(wsr, kvl + i * 2880, 0, 16);
      unsigned np = 0;
#pragma unroll
      for (int i = 0; i < 8; ++i) {
        unsigned m = umax3(pt[i][0], pt[i][1], pt[i][2]);
        m = m > pt[i][3] ? m : pt[i][3];
        np |= __any(m == SENT) ? 1u << i : 0u;
      }
      pend = np;
      if (!pend) break;
      __builtin_amdgcn_s_sleep(1);
    }
    STAMP(14);
    float s[8];
    const unsigned selLo = (unsigned)opq(0x00003F80), selHi = (unsigned)opq(0x3F800000);
#pragma unroll
    for (int j = 0; j < 8; ++j) s[j] = 0.f;
#pragma unroll
    for (int i = 0; i < 8; ++i) {
#pragma unroll
      for (int j = 0; j < 4; ++j) {
        s[2 * j] = dot2(pt[i][j], selLo, s[2 * j]);
        s[2 * j + 1] = dot2(pt[i][j], selHi, s[2 * j + 1]);
      }
    }
    if (u < 180) *(LAS u32x4*)(L + L_KVT + u * 16) = u32x4{pk2(s[0], s[1]), pk2(s[2], s[3]), pk2(s[4], s[5]), pk2(s[6], s[7])};
  }
  waitv<0>();
  STAMP(15);
  bar();
}

template <bool ISA>
DEV void body(const char* XA, const char* WA, const char* XB, const char* W, const char* PHI, char* OUT, char* WS, LAS char* L, int xcd, int jx, u64& tag) {
  const int br = ISA ? 0 : 1;
  const int rt = (xcd >> 1) * 2 + (jx >> 3), hp = (xcd & 1) * 8 + (jx & 7);
  const int tid = threadIdx.x;
  const int wid = __builtin_amdgcn_readfirstlane(tid >> 6);
  int lane = tid & 63;
  int g = lane >> 4, c = lane & 15;
  RSTAMP(30); STAMP(0);

  u64 jb; unsigned jo;
  const u64 pxa = (u64)XA, pwa = (u64)WA, pxb = (u64)XB, pwb = (u64)W;
#define JOB(N) do { \
    int n_ = (N); n_ = n_ < 413 ? n_ : 413; \
    const int b2 = !ISA && n_ >= 207, r = n_ - 207 * b2, st = (r * 2850) >> 16, p = r - 23 * st; \
    const int q = (p - 8) * 64 + lane, kr = (q * 4370) >> 16, r15 = q - 15 * kr, m = (r15 * 13) >> 6, kq = r15 - 5 * m; \
    const u64 px = b2 ? pxb : pxa, pw = b2 ? pwb : pwa; \
    jb = (p < 8 ? px : pw) + (u64)(unsigned)(p < 8 ? ((xcd >> 1) * 512 + p * 64) * 1152 + st * 128 : st * (64 * 3456) + (xcd & 1) * 512); \
    jo = (unsigned)(p < 8 ? lane * 1152 : kr * 3456 + m * 1152 + kq * 128); \
  } while (0)
  auto warm = [&]() {
    asm volatile("s_mov_b32 m0, %2\n\ts_nop 0\n\tglobal_load_lds_dword %0, %1" ::"v"(jo), "s"(jb), "s"(L_DUMMY) : "memory", "m0");
  };
  auto warmw = [&](int w) {
    asm volatile("s_cmp_eq_u32 %3, %4\n\ts_cselect_b64 exec, -1, 0\n\ts_setprio 3\n\ts_mov_b32 m0, %2\n\ts_nop 0\n\tglobal_load_lds_dword %0, %1\n\ts_setprio 0\n\ts_mov_b64 exec, -1"
                 ::"v"(jo), "s"(jb), "s"(L_DUMMY), "s"(wid), "n"(w) : "memory", "m0", "scc");
  };
  constexpr int J1 = 64 + 16 * NRA, NT = (414 - J1 + 15) / 16;
  static_assert(NT - NPRO <= 8 && NPRO <= NT, "");
  if (wid < (ISA ? NRA : 4)) {
    JOB(wid < NRA ? wid * 32 + br * 16 + jx : NRA * 32 + (wid - NRA) * 16 + jx);
    if (wid > 0) __builtin_amdgcn_s_sleep(PFD);
    if (wid > 1) __builtin_amdgcn_s_sleep(PFD);
    if (wid > 2) __builtin_amdgcn_s_sleep(PFD);
    warm();
  }
  if (!ISA) {
    const int t1 = (wid + 4) & 7;
    __builtin_amdgcn_s_sleep(R2SLEEP);
    tag = read_tag(WS);
#pragma unroll
    for (int i = 0; i < 3; ++i)
      if (t1 + 8 * i < NPRO) { JOB(J1 + (t1 + 8 * i) * 16 + jx); warm(); }
    JOB(J1 + (NPRO + ((t1 - NPRO) & 7)) * 16 + jx);
    asm volatile("" : "+v"(jo), "+s"(jb));
  }
  const int dX = 15360 + wid * 4096;
  __amdgpu_buffer_rsrc_t wsr = __builtin_amdgcn_make_buffer_rsrc(WS, 0, W_TOTAL, 0x00020000);
  unsigned phv = 0, phw = 0;
  __amdgpu_buffer_rsrc_t pr = __builtin_amdgcn_make_buffer_rsrc((void*)(PHI + hp * 1296), 0, 1296, 0x00020000);
  int phs = 0;
  if (ISA) {
    int st = tid >= 384, e3 = tid - 384 * st;
    int t = st ? e3 >> 5 : e3 >> 7;
    int a = st ? e3 & 31 : (e3 >> 2) & 31;
    int gg = a >> 3, f = 9 * ((a >> 2) & 1) + 4 * t + (a & 3);
    int d = st ? (gg == 0 ? 16 : 0) : (8 * (gg & 1) + 2 * (gg >> 1) + 2 * (e3 & 3));
    bool ok = (4 * t + (a & 3) < 9) && !(st && (gg & 1)) && tid < 480;
    phs = opq(ok ? (((gg >> 1) * 18 + d) * 18 + f) * 2 : OOB);
  }
  auto phload = [&]() {
    asm volatile("s_setprio 3\n\tbuffer_load_ushort %0, %2, %3, 0 offen\n\tbuffer_load_ushort %1, %2, %3, 0 offen offset:36\n\ts_setprio 0"
                 : "=&v"(phv), "=&v"(phw) : "v"(phs), "s"(pr) : "memory");
  };
  const int obt = W_OB + (br * 8 + rt) * 16 * 18432;
  int obm = obt + 2 * wid * 18432 + hp * 1024 + lane * 16;
  const int xb = ISA ? W_KV + (hp * 8 + rt) * 2880 : W_HB + (rt * 16 + hp) * 2304;
  const int flo = tid == 448 ? W_F + ((br * 8 + rt) * 16 + hp) * 8 : OOB;
  if (ISA) phload();
  if (tid < 16) *(LAS unsigned*)(L + L_ZERO + tid * 4) = (tid & 13) == 12 ? 0x00003F80u : 0u;

  int tq = (lane >> 2) & 3, tp = lane & 3;
  int wrow = (16 * (g >> 1) + 4 * (g & 1) + tq) * 160;
  int unit = 16 * (tp & 1) + 40 * (tp >> 1);
  const int wa = wrow + unit + (hp & 1) * 8;
  const int wx0 = wrow + 32 + (hp & 1) * 8 + (tp == 1 ? 80 : tp == 2 ? 10240 : 0);
  const int wx1 = wx0 + (tp == 2 ? 80 : 5120);
  const int rto = xcd, hf = jx >> 3, cg = jx & 7;

  f32x4 acc[7][2];
#pragma unroll
  for (int t = 0; t < 7; ++t) { acc[t][0] = f32x4{0.f, 0.f, 0.f, 0.f}; acc[t][1] = acc[t][0]; }

  STAMP(1);
  bar();
  STAMP(2); RSTAMP(25);
  bf16x8 wf[2][7];
  u32x4 xf[2][2];
  int wa_s[3], wx_s[3][2], xa_s[3];
#pragma unroll
  for (int i = 0; i < 3; ++i) {
    wa_s[i] = opq(wa + i * SLOT); wx_s[i][0] = opq(wx0 + i * SLOT); wx_s[i][1] = opq(wx1 + i * SLOT);
    xa_s[i] = opq(i * SLOT + dX + c * 128 + ((g ^ (c >> 1)) * 16));
  }
  auto rdW = [&](auto KN, auto T) {
    constexpr int kn = KN, t = T, sl = (kn >> 1) % 3, od = kn & 1;
    if constexpr (t < 4) return trfrag(L, wa_s[sl] + od * 5120 + (t >> 1) * 80 + (t & 1) * 8);
    else if constexpr (t < 6) return trfrag(L, wa_s[sl] + 10240 + od * 80 + (t & 1) * 8);
    else return trfrag(L, wx_s[sl][od]);
  };
  auto rdX = [&](auto KN, int mt) {
    constexpr int kn = KN, sl = (kn >> 1) % 3, od = kn & 1;
    return lds128(L, (od ? xa_s[sl] ^ 64 : xa_s[sl]) + mt * 2048);
  };
  using I0 = std::integral_constant<int, 0>;
  xf[0][0] = rdX(I0{}, 0); xf[0][1] = rdX(I0{}, 1);
  sfor<7>([&](auto T) { wf[0][T] = rdW(I0{}, T); });
  sfor<18>([&](auto K) {
    constexpr int k = K, cur = k & 1, nxt = cur ^ 1, kn = k + 1;
    using KN = std::integral_constant<int, kn>;
    if constexpr (ISA && k == KW) waitv<0>();
    if constexpr ((k & 1) && kn < 18) bar();
    if constexpr (ISA && k == KW) {
      asm volatile("" : "+v"(phv), "+v"(phw));
      if (tid < 480) *(LAS unsigned*)(L + L_PT + tid * 4) = phv | (phw << 16);
    }
    sfor<7>([&](auto T) {
      constexpr int t = T;
      SB();
      acc[t][0] = mfma(wf[cur][t], asb(xf[cur][0]), acc[t][0]);
      if constexpr (kn < 18) {
        if constexpr (t == 0) { xf[nxt][0] = rdX(KN{}, 0); xf[nxt][1] = rdX(KN{}, 1); }
        wf[nxt][t] = rdW(KN{}, T);
      }
      SB();
      acc[t][1] = mfma(wf[cur][t], asb(xf[cur][1]), acc[t][1]);
      if constexpr (!ISA && k == 0 && t == 0) __builtin_amdgcn_raw_buffer_store_b64(u32x2{(unsigned)tag, (unsigned)(tag >> 32)}, wsr, flo, 0, 16);
      if constexpr (!ISA && t == 1 && k >= J3K && (k - J3K) % J3S == 0 && NPRO + (k - J3K) / J3S < NT) warmw((4 + NPRO + (k - J3K) / J3S) & 7);
    });
    SB();
    if constexpr (k == 5 || k == 11) STAMP(3 + (k == 11));
  });
  STAMP(5); RSTAMP(26);
  lane = opq(tid) & 63; g = lane >> 4; c = lane & 15; tq = (lane >> 2) & 3; tp = lane & 3;
  const bool act = (c >> 3) == (g >> 1);
  const int pta = L_PT + (g * 8 + (c & 7)) * 16;
  const int ptb = L_PT + 1536 + (g * 8 + (c & 7)) * 4;

  u32x4 km[2], qm[2];
  unsigned ex[2], qx[2];
#pragma unroll
  for (int mt = 0; mt < 2; ++mt) {
    km[mt] = u32x4{pk2(acc[0][mt][0], acc[0][mt][1]), pk2(acc[0][mt][2], acc[0][mt][3]), pk2(acc[1][mt][0], acc[1][mt][1]), pk2(acc[1][mt][2], acc[1][mt][3])};
    qm[mt] = u32x4{pk2(acc[4][mt][0], acc[4][mt][1]), pk2(acc[4][mt][2], acc[4][mt][3]), pk2(acc[5][mt][0], acc[5][mt][1]), pk2(acc[5][mt][2], acc[5][mt][3])};
    ex[mt] = pk2(acc[6][mt][0], acc[6][mt][1]);
    qx[mt] = pk2(acc[6][mt][2], acc[6][mt][3]);
    if (!ISA && (g & 1)) {
      ex[mt] = pk2(acc[6][0][mt], acc[6][1][mt]);
      qx[mt] = pk2(acc[6][0][2 + mt], acc[6][1][2 + mt]);
    }
    swap32(ex[mt], qx[mt]);
    qx[mt] = (g & 1) ? 0u : qx[mt];
  }
  u32x4 om[2]; unsigned ox[2];
  if (ISA) {
    bar();
    STAMP(6);
    const int rbase = L_REC + g * 12288;
    const int pc = g & 1;
#pragma unroll
    for (int mt = 0; mt < 2; ++mt) {
      const int rp = 32 * wid + 16 * mt + c;
      *(LAS u32x4*)(L + rbase + 8192 + (rp ^ (8 * pc)) * 16) =
          u32x4{pk2(acc[2][mt][0], acc[2][mt][1]), pk2(acc[2][mt][2], acc[2][mt][3]), pk2(acc[3][mt][0], acc[3][mt][1]), pk2(acc[3][mt][2], acc[3][mt][3])};
    }
    {
      unsigned ph[2][9];
      unsigned ek[2] = {pc ? 0u : ex[0], pc ? 0u : ex[1]};
      features(L, pta, ptb, act, km, ek, ph, [&](int mt) {
        const int rp = 32 * wid + 16 * mt + c;
        *(LAS u32x4*)(L + rbase + rp * 16) = u32x4{ph[mt][0], ph[mt][1], ph[mt][2], ph[mt][3]};
        *(LAS u32x4*)(L + rbase + 4096 + (rp ^ 8) * 16) = u32x4{ph[mt][4], ph[mt][5], ph[mt][6], ph[mt][7]};
        *(LAS u32x2*)(L + L_REC + 49152 + g * 2112 + rp * 8) = u32x2{ph[mt][8], ex[mt]};
      });
    }
    STAMP(7);
    bar();
    STAMP(8);
    {
      const int hd = wid & 1, nt = (wid >> 1) & 1;
      const int rowb = 16 * (g >> 1) + 4 * (g & 1) + tq;
      const int hb = L_REC + hd * 2 * 12288;
      const int sel = tp >> 1;
      const int aM0 = hb + (tp & 1) * 8 + sel * 4096 + (rowb + 8 * sel) * 16;
      const int aN0 = hb + (tp & 1) * 8 + 8192 + sel * 12288 + (rowb + 8 * sel) * 16;
      const int aS = L_REC + 49152 + (2 * hd + (tp & 1)) * 2112 + rowb * 8;
      const int a = (c >> 3) * 8 + (c & 7);
      const int b = (c >= 2 && c < 6) ? 14 + c : -1;
      const int dI = nt == 0 ? a : b;
      const int n0 = nt == 0 ? aN0 : (tp == 0 ? aS + 2112 : L_ZERO + 48);
      const int n1 = nt == 0 ? aN0 ^ 128 : (tp == 0 ? n0 + 64 : n0);
      const int nst = nt == 0 ? 512 : (tp == 0 ? 256 : 0);
      auto tr2 = [&](int p, int q) {
        s16x4 u = __builtin_amdgcn_ds_read_tr16_b64_v4i16((LAS s16x4*)(L + p));
        s16x4 v = __builtin_amdgcn_ds_read_tr16_b64_v4i16((LAS s16x4*)(L + q));
        u32x2 ua = __builtin_bit_cast(u32x2, u), ub = __builtin_bit_cast(u32x2, v);
        return asb(u32x4{ua.x, ua.y, ub.x, ub.y});
      };
      f32x4 D0 = {0.f, 0.f, 0.f, 0.f}, D1 = D0;
      bf16x8 nf[8];
#pragma unroll
      for (int ks = 0; ks < 8; ++ks) nf[ks] = tr2(n0 + ks * nst, n1 + ks * nst);
      if (wid < 4) {
        bf16x8 m0[8], m1[8];
#pragma unroll
        for (int ks = 0; ks < 8; ++ks) {
          m0[ks] = tr2(aM0 + ks * 512, (aM0 ^ 128) + ks * 512);
          m1[ks] = tr2(aM0 + 12288 + ks * 512, (aM0 ^ 128) + 12288 + ks * 512);
        }
#pragma unroll
        for (int ks = 0; ks < 8; ++ks) { D0 = mfma(m0[ks], nf[ks], D0); D1 = mfma(m1[ks], nf[ks], D1); }
        unsigned a0 = pk2(D0[0], D0[1]), a1 = pk2(D0[2], D0[3]), b0 = pk2(D1[0], D1[1]), b1 = pk2(D1[2], D1[3]);
        swap16(a0, b0); swap16(a1, b1);
        const int kvm = dI >= 0 ? xb + hd * 1440 + dI * 64 + (((g & 1) << 1) | (g >> 1)) * 16 : OOB;
        __builtin_amdgcn_raw_buffer_store_b128(canon4(u32x4{a0, a1, b0, b1}), wsr, kvm, 0, 16);
      } else {
        bf16x8 m0[8];
#pragma unroll
        for (int ks = 0; ks < 8; ++ks) m0[ks] = tr2(aS + ks * 256, aS + 64 + ks * 256);
#pragma unroll
        for (int ks = 0; ks < 8; ks += 2) { D0 = mfma(m0[ks], nf[ks], D0); D1 = mfma(m0[ks + 1], nf[ks + 1], D1); }
        unsigned t0 = pk2(D0[0] + D1[0], D0[1] + D1[1]), t1 = t0;
        swap16(t0, t1);
        const int kvx = (dI >= 0 && g == 0) ? xb + hd * 1440 + 1280 + dI * 8 : OOB;
        __builtin_amdgcn_raw_buffer_store_b64(u32x2{canon(t0), canon(t1)}, wsr, kvx, 0, 16);
      }
    }
    STAMP(9);
    unsigned ph[2][9];
    features(L, pta, ptb, act, qm, qx, ph, [&](int) {});
    STAMP(13);
    bar();
    STAMP(15);
    const int hb = L_KVT + (g >> 1) * 1440;
    f32x4 O[2][3];
#pragma unroll
    for (int t = 0; t < 3; ++t) {
      const int d = t < 2 ? ((c >> 2) & 1) * 8 + 4 * t + (c & 3) : 16 + (c & 3);
      const int a0 = act ? hb + d * 64 + (g & 1) * 32 : L_ZERO;
      const int a2 = act ? hb + 1280 + d * 8 + (g & 1) * 4 : L_ZERO;
      u32x4 T0 = lds128(L, a0), T1 = lds128(L, a0 + 16);
      u32x4 T2 = {lds32(L, a2), 0u, 0u, 0u};
#pragma unroll
      for (int mt = 0; mt < 2; ++mt) {
        f32x4 z = {0.f, 0.f, 0.f, 0.f};
        z = mfma(asb(T0), asb(u32x4{ph[mt][0], ph[mt][1], ph[mt][2], ph[mt][3]}), z);
        z = mfma(asb(T1), asb(u32x4{ph[mt][4], ph[mt][5], ph[mt][6], ph[mt][7]}), z);
        O[mt][t] = mfma(asb(T2), asb(u32x4{ph[mt][8], 0u, 0u, 0u}), z);
      }
    }
#pragma unroll
    for (int mt = 0; mt < 2; ++mt) {
      float rd = __builtin_amdgcn_rcpf(O[mt][2][2] + 1e-6f);
      om[mt] = u32x4{pk2(O[mt][0][0] * rd, O[mt][0][1] * rd), pk2(O[mt][0][2] * rd, O[mt][0][3] * rd), pk2(O[mt][1][0] * rd, O[mt][1][1] * rd), pk2(O[mt][1][2] * rd, O[mt][1][3] * rd)};
      ox[mt] = pk2(O[mt][2][0] * rd, O[mt][2][1] * rd);
    }
  } else {
    u32x4 own[4]; u32x2 ownx;
    const int rec = g * 144 + 8 + 16 * wid + c;
    own[0] = km[0]; own[1] = km[1];
    own[2] = u32x4{pk2(acc[2][0][0], acc[2][1][0]), pk2(acc[2][0][1], acc[2][1][1]), pk2(acc[2][0][2], acc[2][1][2]), pk2(acc[2][0][3], acc[2][1][3])};
    own[3] = u32x4{pk2(acc[3][0][0], acc[3][1][0]), pk2(acc[3][0][1], acc[3][1][1]), pk2(acc[3][0][2], acc[3][1][2]), pk2(acc[3][0][3], acc[3][1][3])};
    ownx = u32x2{ex[0], ex[1]};
    if (wid == 7) {
      const int ho = c >= 8 ? xb + (g * 8 + c - 8) * 16 : OOB;
#pragma unroll
      for (int q = 0; q < 4; ++q) __builtin_amdgcn_raw_buffer_store_b128(canon4(own[q]), wsr, ho + q * 512, 0, 16);
      __builtin_amdgcn_raw_buffer_store_b64(u32x2{canon(ownx[0]), canon(ownx[1])}, wsr, c >= 8 ? xb + 2048 + (g * 8 + c - 8) * 8 : OOB, 0, 16);
    }
    bar();
    STAMP(6);
#pragma unroll
    for (int q = 0; q < 4; ++q) *(LAS u32x4*)(L + L_REC + q * 9216 + rec * 16) = own[q];
    *(LAS u32x2*)(L + L_REC + 36864 + rec * 8) = ownx;
    STAMP(7);
    bar();
    STAMP(8);
    if (wid == 0) {
      u32x4 hq[4]; u32x2 hx;
      if (rt > 0) {
        const int hs = W_HB + ((rt - 1) * 16 + hp) * 2304, hl = g * 8 + (c & 7);
#pragma unroll 1
        for (int spin = 0; spin < SPIN_CAP; ++spin) {
          unsigned m = 0;
#pragma unroll
          for (int q = 0; q < 4; ++q) hq[q] = __builtin_amdgcn_raw_buffer_load_b128(wsr, hs + q * 512 + hl * 16, 0, 16);
          hx = __builtin_amdgcn_raw_buffer_load_b64(wsr, hs + 2048 + hl * 8, 0, 16);
#pragma unroll
          for (int q = 0; q < 4; ++q) { m = umax3(m, hq[q][0], hq[q][1]); m = umax3(m, hq[q][2], hq[q][3]); }
          m = umax3(m, hx[0], hx[1]);
          if (__all(m != SENT)) break;
          __builtin_amdgcn_s_sleep(1);
        }
      } else {
        const int r0 = g * 144 + 8;
        hq[0] = lds128(L, L_REC + r0 * 16); hq[1] = hq[0];
        u32x4 a = lds128(L, L_REC + 2 * 9216 + r0 * 16), b = lds128(L, L_REC + 3 * 9216 + r0 * 16);
        u32x2 e = lds64(L, L_REC + 36864 + r0 * 8);
#pragma unroll
        for (int j = 0; j < 4; ++j) { hq[2][j] = (a[j] & 0xFFFFu) * 0x10001u; hq[3][j] = (b[j] & 0xFFFFu) * 0x10001u; }
        hx = (g & 1) ? u32x2{(e[0] & 0xFFFFu) * 0x10001u, (e[1] & 0xFFFFu) * 0x10001u} : u32x2{e[0], e[0]};
      }
      const int hr = g * 144 + (c & 7);
#pragma unroll
      for (int q = 0; q < 4; ++q) *(LAS u32x4*)(L + L_REC + q * 9216 + hr * 16) = hq[q];
      *(LAS u32x2*)(L + L_REC + 36864 + hr * 8) = hx;
    }
    STAMP(13);
    u32x4 R[5][4]; u32x2 RX[5];
#pragma unroll
    for (int q = 0; q < 4; ++q) R[0][q] = own[q];
    RX[0] = ownx;
#pragma unroll
    for (int j = 1; j < 5; ++j) {
#pragma unroll
      for (int q = 0; q < 4; ++q) if (!(j == 4 && q == 0)) R[j][q] = lds128(L, L_REC + q * 9216 + (rec - 2 * j) * 16);
      RX[j] = lds64(L, L_REC + 36864 + (rec - 2 * j) * 8);
    }
    auto qk = [&](int mt, int j, int lh) {
      float a = dot2(qx[mt], RX[j][lh], 0.f);
#pragma unroll
      for (int d = 0; d < 4; ++d) a = dot2(qm[mt][d], R[j][lh][d], a);
      return a;
    };
    float lg[8];
#pragma unroll
    for (int sl = 0; sl < 8; ++sl) {
      float a = sl == 0 ? qk(0, 4, 1) : qk(0, sl >> 1, (sl & 1) ^ 1);
      float b = sl == 0 ? qk(1, 0, 1) : qk(1, sl >> 1, (sl & 1) ^ 1);
      swap16f(a, b);
      lg[sl] = (a + b) * 0.23570226039551584f;
    }
    if (rt == 0 && wid == 0) {
      const int i = c >> 1;
#pragma unroll
      for (int sl = 0; sl < 8; ++sl) {
        int j = sl == 0 ? ((g & 1) ? 0 : 4) : (sl >> 1);
        lg[sl] = i < j ? -1e9f : lg[sl];
      }
    }
    float mx = lg[0];
#pragma unroll
    for (int sl = 1; sl < 8; ++sl) mx = __builtin_fmaxf(mx, lg[sl]);
    const float LOG2E = 1.4426950408889634f;
    float sum = 0.f;
#pragma unroll
    for (int sl = 0; sl < 8; ++sl) { lg[sl] = __builtin_amdgcn_exp2f((lg[sl] - mx) * LOG2E); sum += lg[sl]; }
    float rs = __builtin_amdgcn_rcpf(sum);
    unsigned ul[5], uh[4];
#pragma unroll
    for (int j = 0; j < 4; ++j) {
      unsigned a = pk2(lg[2 * j + 1] * rs, lg[2 * j] * rs), b = a;
      swap16(a, b);
      ul[j] = a; uh[j] = b;
    }
    ul[4] = ul[0] & 0xFFFF0000u; ul[0] &= 0x0000FFFFu;
    float ol[10], oh[10];
#pragma unroll
    for (int d = 0; d < 10; ++d) {
      float a = 0.f, b = 0.f;
#pragma unroll
      for (int j = 0; j < 5; ++j) {
        unsigned v = d < 8 ? R[j][2 + (d >> 2)][d & 3] : RX[j][d - 8];
        a = dot2(ul[j], v, a);
        if (j < 4) b = dot2(uh[j], v, b);
      }
      ol[d] = a; oh[d] = b;
    }
    om[0] = u32x4{pk2(ol[0], ol[1]), pk2(ol[2], ol[3]), pk2(ol[4], ol[5]), pk2(ol[6], ol[7])};
    om[1] = u32x4{pk2(oh[0], oh[1]), pk2(oh[2], oh[3]), pk2(oh[4], oh[5]), pk2(oh[6], oh[7])};
    ox[0] = pk2(ol[8], ol[9]); ox[1] = pk2(oh[8], oh[9]);
  }
  STAMP(16); RSTAMP(27);
  lane = opq(tid) & 63; g = lane >> 4; c = lane & 15; tq = (lane >> 2) & 3; tp = lane & 3;
  obm = obt + 2 * wid * 18432 + hp * 1024 + lane * 16;
  wrow = (16 * (g >> 1) + 4 * (g & 1) + tq) * 160;
  unit = 16 * (tp & 1) + 40 * (tp >> 1);
  {
    const int obr = g == 1 ? obt + 2 * wid * 18432 + 16384 + hp * 128 + c * 8 : OOB;
#pragma unroll
    for (int mt = 0; mt < 2; ++mt) __builtin_amdgcn_raw_buffer_store_b128(canon4(om[mt]), wsr, obm + mt * 18432, 0, 16);
#pragma unroll
    for (int mt = 0; mt < 2; ++mt) {
      unsigned t0 = ox[mt], t1 = t0;
      swap32(t0, t1);
      __builtin_amdgcn_raw_buffer_store_b64(u32x2{canon(t0), canon(t1)}, wsr, obr + mt * 18432, 0, 16);
    }
  }
  if (!ISA) bar();
  STAMP(17);

  {
    const int tile = hf * 8 + wid;
    const int ob = W_OB + ((br * 8 + rto) * 16 + tile) * 18432;
    u32x4 of[18];
    const int oa_m = ob + lane * 16, oa_r = ob + 16384 + 2 * g * 128 + c * 8;
#pragma unroll 1
    for (int spin = 0; spin < SPIN_CAP; ++spin) {
      unsigned m = 0;
#pragma unroll
      for (int i = 0; i < 2; ++i) {
        u32x2 a = __builtin_amdgcn_raw_buffer_load_b64(wsr, oa_r + i * 1024, 0, 16);
        u32x2 b = __builtin_amdgcn_raw_buffer_load_b64(wsr, oa_r + i * 1024 + 128, 0, 16);
        of[16 + i] = u32x4{a[0], a[1], b[0], b[1]};
      }
#pragma unroll
      for (int i = 16; i < 18; ++i) { m = umax3(m, of[i][0], of[i][1]); m = umax3(m, of[i][2], of[i][3]); }
      if (__all(m != SENT)) break;
      __builtin_amdgcn_s_sleep(GSLEEP);
    }
    STAMP(18); RSTAMP(28);
    const int oa = wrow + unit + L_WO;
    const int os = wrow + ((tp & 1) ? 13 : 4) * 8 + L_WO;
    f32x4 C[5];
#pragma unroll 1
    for (int spin = 0; spin < SPIN_CAP; ++spin) {
#pragma unroll
      for (int i = 0; i < 16; ++i) of[i] = __builtin_amdgcn_raw_buffer_load_b128(wsr, oa_m + i * 1024, 0, 16);
      SB();
#pragma unroll
      for (int t = 0; t < 5; ++t) C[t] = f32x4{0.f, 0.f, 0.f, 0.f};
      const int oa_l = opq(oa), os_l = opq(os), oa_h = opq(oa + 46080), os_h = opq(os + 46080);
      auto rdO = [&](auto J, auto T) {
        constexpr int kk = (J + 16) % 18, t = T, no = (kk % 9) * 5120;
        if constexpr (t < 4) return trfrag(L, (kk < 9 ? oa_l : oa_h) + no + (t >> 1) * 72 + (t & 1) * 8);
        else return trfrag(L, (kk < 9 ? os_l : os_h) + no);
      };
      sfor<5>([&](auto T) { wf[0][T] = rdO(I0{}, T); });
      unsigned m = 0;
      sfor<18>([&](auto J) {
        constexpr int j = J, cur = j & 1, nxt = cur ^ 1, kk = (j + 16) % 18;
        sfor<5>([&](auto T) {
          SB();
          C[T] = mfma(wf[cur][T], asb(of[kk]), C[T]);
          if constexpr (j < 17) wf[nxt][T] = rdO(std::integral_constant<int, j + 1>{}, T);
          if constexpr (T == 1 && kk < 16) m = umax3(m, of[kk][0], of[kk][1]);
          if constexpr (T == 2 && kk < 16) m = umax3(m, of[kk][2], of[kk][3]);
        });
        SB();
      });
      if (__all(m != SENT)) break;
    }
    STAMP(19); RSTAMP(29);
    const int row = rto * 256 + 32 * (tile >> 1) + 4 * (c >> 1) + (c & 1) + 2 * (tile & 1);
    char* o = OUT + (size_t)row * 2304 + br * 1152 + cg * 144;
    const int cu = 16 * (g & 1) + 40 * (g >> 1);
    typedef unsigned u32x4a __attribute__((ext_vector_type(4), aligned(8)));
    *(u32x4a*)(o + cu) = u32x4{pk2(C[0][0], C[0][1]), pk2(C[0][2], C[0][3]), pk2(C[1][0], C[1][1]), pk2(C[1][2], C[1][3])};
    *(u32x4a*)(o + cu + 72) = u32x4{pk2(C[2][0], C[2][1]), pk2(C[2][2], C[2][3]), pk2(C[3][0], C[3][1]), pk2(C[3][2], C[3][3])};
    if (g < 2) *(u32x2*)(o + (g ? 13 : 4) * 8) = u32x2{pk2(C[4][0], C[4][1]), pk2(C[4][2], C[4][3])};
  }
}

__global__ __launch_bounds__(768, 3) void fused(Params P) {
  extern __shared__ __attribute__((aligned(16))) char lds_raw[];
  LAS char* L = (LAS char*)lds_raw;
  const int b = blockIdx.x, xcd = b & 7, br = b >> 7, jx = (b >> 3) & 15;
  const char* XA = P.x[0];
  const char* WA = P.w[0];
  const char* X = br ? P.x[1] : P.x[0];
  const char* W = br ? P.w[1] : P.w[0];
  const char* WO = br ? P.wo[1] : P.wo[0];
  const char* PHI = P.wphi;
  char* OUT = P.out;
  char* WS = P.ws;
  asm volatile("" : "+s"(X), "+s"(W), "+s"(WO), "+s"(PHI), "+s"(OUT), "+s"(WS), "+s"(XA), "+s"(WA));
  u64 tag = 0;
  const int wid = __builtin_amdgcn_readfirstlane(threadIdx.x >> 6);
  if (wid >= 8) {
    if (br == 0) loader<true>(X, W, WO, WS, L, xcd, jx, wid);
    else loader<false>(X, W, WO, WS, L, xcd, jx, wid);
    return;
  }
  if (br == 0) body<true>(X, W, X, W, PHI, OUT, WS, L, xcd, jx, tag);
  else body<false>(XA, WA, X, W, PHI, OUT, WS, L, xcd, jx, tag);
  if (b == 255 && threadIdx.x < 64) {
    const GAS u64* f = (const GAS u64*)(WS + W_F) + threadIdx.x;
#pragma unroll 1
    for (int spin = 0; spin < SPIN_CAP; ++spin) {
      bool e = true;
#pragma unroll
      for (int i = 0; i < 4; ++i) e &= __hip_atomic_load(f + 64 * i, __ATOMIC_RELAXED, __HIP_MEMORY_SCOPE_AGENT) == tag;
      if (__all(e)) break;
      __builtin_amdgcn_s_sleep(2);
    }
    if (threadIdx.x == 0) __hip_atomic_store((GAS u64*)(WS + W_EPOCH), tag, __ATOMIC_RELAXED, __HIP_MEMORY_SCOPE_AGENT);
  }
}

extern "C" void kernel_launch(void* const* d_in, const int* in_sizes, int n_in, void* d_out, int out_size, void* d_ws,
                              size_t ws_size, hipStream_t stream) {
  Params P;
  P.x[0] = (const char*)d_in[0]; P.w[0] = (const char*)d_in[1]; P.wphi = (const char*)d_in[2]; P.wo[0] = (const char*)d_in[3];
  P.x[1] = (const char*)d_in[4]; P.w[1] = (const char*)d_in[5]; P.wo[1] = (const char*)d_in[6];
  P.out = (char*)d_out; P.ws = (char*)d_ws;
  (void)hipFuncSetAttribute((const void*)fused, hipFuncAttributeMaxDynamicSharedMemorySize, LDS_BYTES);
  hipLaunchKernelGGL(fused, dim3(256), dim3(768), LDS_BYTES, stream, P);
}
```

```cpp
#include <hip/hip_runtime.h>
#include <stdint.h>
#include <type_traits>
#pragma clang diagnostic ignored "-Winline-asm"

typedef short s16x4 __attribute__((ext_vector_type(4)));
typedef __bf16 bf16x8 __attribute__((ext_vector_type(8)));
typedef __bf16 bf16x2 __attribute__((ext_vector_type(2)));
typedef float f32x4 __attribute__((ext_vector_type(4)));
typedef float f32x2 __attribute__((ext_vector_type(2)));
typedef unsigned u32x4 __attribute__((ext_vector_type(4)));
typedef unsigned u32x2 __attribute__((ext_vector_type(2)));
typedef unsigned long long u64;
#define LAS __attribute__((address_space(3)))
#define GAS __attribute__((address_space(1)))
#define DEV __device__ __forceinline__
#define SB() __builtin_amdgcn_sched_barrier(0)

constexpr int SLOT = 50176;
constexpr int L_REC = 0;
constexpr int L_WO = 57600;
constexpr int L_ZERO = 3 * SLOT;
constexpr int L_PT = L_ZERO + 64;
constexpr int L_KVT = L_PT + 1920;
constexpr int L_DUMMY = L_KVT + 2880;
constexpr int L_END = L_DUMMY + 256;
static_assert(L_END + 3072 <= 162816 && L_WO + 92160 <= L_ZERO && L_WO + 30720 <= 2 * SLOT, "lds");
constexpr int LDS_BYTES = 163840;

constexpr int W_EPOCH = 0;
constexpr int W_F = 4096;
constexpr int W_KV = 65536;
constexpr int W_OB = 1048576;
constexpr int W_TOTAL = W_OB + 2 * 8 * 16 * 18432;
constexpr unsigned SENT = 0xFFFFFFFFu;
constexpr int OOB = 0x40000000;
constexpr int SPIN_CAP = 400000;
#define KVSLEEP 24
#define LDSLEEP 8
#define BLDSLEEP 30
#define BTH 0
#define BTN 4
#define NPRO 12
#define R2SLEEP 20
#define NRA 2
#define PFD 4
#define J3K 0
#define J3S 1
#define KW 3
#define LEAD0 3
#define LEADNA 2
#define LEADNB 2
#define LEADA 4
#define LEADB 4
#define HP 12
#define NWO1A 0
#define NWO1B 0
#define WSLA 0
#define WSLB 0
#define WS0A 0
#define WS0B 0
#define JB 0
#define CSLA 28
#define CSLB 24
#define GD 4
#define HX 0
#define AJ 1
#define NWO2A 12
#define GSLEEP 2

template <int N> DEV void waitv() { asm volatile("s_waitcnt vmcnt(%0)" ::"n"(N) : "memory"); }
DEV void bar() { asm volatile("s_waitcnt lgkmcnt(0)\n\ts_barrier" ::: "memory"); }
template <int N> DEV void waitbar() { asm volatile("s_waitcnt vmcnt(%0)\n\ts_barrier" ::"n"(N) : "memory"); }

DEV unsigned pk2(float a, float b) {
  f32x2 v = {a, b};
  bf16x2 r = __builtin_convertvector(v, bf16x2);
  return __builtin_bit_cast(unsigned, r);
}
DEV bf16x8 asb(u32x4 v) { return __builtin_bit_cast(bf16x8, v); }
DEV f32x4 mfma(bf16x8 a, bf16x8 b, f32x4 c) { return __builtin_amdgcn_mfma_f32_16x16x32_bf16(a, b, c, 0, 0, 0); }
DEV float dot2(unsigned a, unsigned b, float c) {
  return __builtin_amdgcn_fdot2_f32_bf16(__builtin_bit_cast(bf16x2, a), __builtin_bit_cast(bf16x2, b), c, false);
}
DEV u32x4 lds128(LAS char* L, int off) { return *(LAS u32x4*)(L + off); }
DEV u32x2 lds64(LAS char* L, int off) { return *(LAS u32x2*)(L + off); }
DEV unsigned lds32(LAS char* L, int off) { return *(LAS unsigned*)(L + off); }
DEV bf16x8 trfrag(LAS char* L, int off) {
  s16x4 a = __builtin_amdgcn_ds_read_tr16_b64_v4i16((LAS s16x4*)(L + off));
  s16x4 b = __builtin_amdgcn_ds_read_tr16_b64_v4i16((LAS s16x4*)(L + off + 1280));
  u32x2 ua = __builtin_bit_cast(u32x2, a), ub = __builtin_bit_cast(u32x2, b);
  u32x4 r = {ua.x, ua.y, ub.x, ub.y};
  return asb(r);
}
DEV void glds60(const char* base, unsigned voff, int dst) {
  asm volatile("s_mov_b32 m0, %2\n\ts_lshr_b64 exec, -1, 4\n\tglobal_load_lds_dwordx4 %0, %1\n\ts_mov_b64 exec, -1"
               ::"v"(voff), "s"(base), "s"(dst) : "memory", "m0", "scc");
}
DEV void glds64(const char* base, unsigned voff, int dst) {
  asm volatile("s_mov_b32 m0, %2\n\ts_nop 0\n\tglobal_load_lds_dwordx4 %0, %1" ::"v"(voff), "s"(base), "s"(dst) : "memory", "m0");
}
DEV void glds32(const char* base, unsigned voff, int dst) {
  asm volatile("s_mov_b32 m0, %2\n\ts_lshr_b64 exec, -1, 32\n\tglobal_load_lds_dwordx4 %0, %1\n\ts_mov_b64 exec, -1"
               ::"v"(voff), "s"(base), "s"(dst) : "memory", "m0", "scc");
}
template <int OFF> DEV void gload128(u32x4& d, const char* base, unsigned voff) {
  asm volatile("global_load_dwordx4 %0, %1, %2 offset:%3" : "=v"(d) : "v"(voff), "s"(base), "n"(OFF) : "memory");
}
DEV void gload64s(u32x2& d, const char* base, unsigned voff) {
  asm volatile("global_load_dwordx2 %0, %1, %2 sc1" : "=v"(d) : "v"(voff), "s"(base) : "memory");
}
#define STAMP(i) do {} while (0)
#define RSTAMP(i) do {} while (0)
DEV int opq(int v) { asm volatile("" : "+v"(v)); return v; }
DEV void swap16(unsigned& a, unsigned& b) {
  auto r = __builtin_amdgcn_permlane16_swap(a, b, false, false);
  a = r[0]; b = r[1];
}
DEV void swap32(unsigned& a, unsigned& b) {
  auto r = __builtin_amdgcn_permlane32_swap(a, b, false, false);
  a = r[0]; b = r[1];
}
DEV void swap16f(float& a, float& b) {
  unsigned x = __float_as_uint(a), y = __float_as_uint(b);
  swap16(x, y);
  a = __uint_as_float(x); b = __uint_as_float(y);
}
DEV unsigned canon(unsigned x) { return x < 0xFFFFFFFEu ? x : 0xFFFFFFFEu; }
DEV unsigned umax3(unsigned a, unsigned b, unsigned c) { unsigned m = a > b ? a : b; return m > c ? m : c; }
DEV u32x4 canon4(u32x4 v) { return u32x4{canon(v[0]), canon(v[1]), canon(v[2]), canon(v[3])}; }

struct Params {
  const char* x[2];
  const char* w[2];
  const char* wo[2];
  const char* wphi;
  char* out;
  char* ws;
};

DEV int kmap(int p) { return (p & ~12) | ((p & 4) << 1) | ((p & 8) >> 1); }
DEV int worow(int k) {
  int m = k + 4 * (((k >> 4) + 1) >> 1);
  int kr = k - 512;
  int r = 36 * (kr >> 2) + 16 + (kr & 3);
  return k < 512 ? m : r;
}

template <class F>
DEV void features(LAS char* L, int pta, int ptb, bool act, const u32x4 (&m)[2], const unsigned (&e)[2], unsigned (&ph)[2][9], F&& done) {
  f32x4 P[2][3];
#pragma unroll
  for (int t = 0; t < 3; ++t) {
    u32x4 T0 = lds128(L, act ? pta + t * 512 : L_ZERO);
    u32x4 T1 = {lds32(L, act ? ptb + t * 128 : L_ZERO), 0u, 0u, 0u};
#pragma unroll
    for (int mt = 0; mt < 2; ++mt) {
      f32x4 z = {0.f, 0.f, 0.f, 0.f};
      u32x4 E = {e[mt], 0u, 0u, 0u};
      z = mfma(asb(T0), asb(m[mt]), z);
      P[mt][t] = mfma(asb(T1), asb(E), z);
    }
  }
  const float LOG2E = 1.4426950408889634f;
  float p[2][9], mx[2], mn[2];
#pragma unroll
  for (int mt = 0; mt < 2; ++mt) {
#pragma unroll
    for (int i = 0; i < 9; ++i) p[mt][i] = P[mt][i >> 2][i & 3];
    float a = p[mt][0], b = p[mt][0];
#pragma unroll
    for (int i = 1; i < 9; ++i) { a = __builtin_fmaxf(a, p[mt][i]); b = __builtin_fminf(b, p[mt][i]); }
    float a2 = a, b2 = b;
    swap16f(a, a2); swap16f(b, b2);
    mx[mt] = __builtin_fmaxf(a, a2) * LOG2E; mn[mt] = __builtin_fminf(b, b2) * LOG2E;
  }
#pragma unroll
  for (int mt = 0; mt < 2; ++mt) {
    float ep[9], en[9], sp = 0.f, sn = 0.f;
#pragma unroll
    for (int i = 0; i < 9; ++i) {
      ep[i] = __builtin_amdgcn_exp2f(__builtin_fmaf(p[mt][i], LOG2E, -mx[mt]));
      en[i] = __builtin_amdgcn_exp2f(__builtin_fmaf(p[mt][i], -LOG2E, mn[mt]));
      sp += ep[i]; sn += en[i];
    }
    float sp2 = sp, sn2 = sn;
    swap16f(sp, sp2); swap16f(sn, sn2);
    float rp = __builtin_amdgcn_rcpf(sp + sp2), rn = __builtin_amdgcn_rcpf(sn + sn2);
    float f[18];
#pragma unroll
    for (int i = 0; i < 9; ++i) { f[i] = ep[i] * rp; f[9 + i] = en[i] * rn; }
#pragma unroll
    for (int i = 0; i < 9; ++i) ph[mt][i] = pk2(f[2 * i], f[2 * i + 1]);
    done(mt);
  }
}

template <int N, class F> DEV void sfor(F&& f) {
  if constexpr (N > 0) { sfor<N - 1>(f); f(std::integral_constant<int, N - 1>{}); }
}

DEV u64 next_tag(u64 t);
DEV u64 read_tag(const char* ws) {
  u64 t;
  asm volatile("s_load_dwordx2 %0, %1, 0x0\n\ts_waitcnt lgkmcnt(0)" : "=s"(t) : "s"(ws) : "memory");
  return next_tag(t);
}
DEV u64 next_tag(u64 t) { return (t ^ 0x9E3779B97F4A7C15ull) * 0xD1342543DE82EF95ull + 0x632BE59BD9B4E019ull; }

constexpr int J1 = 64 + 16 * NRA, NT = (414 - J1 + 15) / 16;
template <bool BOTH>
DEV void job(int n_, u64 pxa, u64 pwa, u64 pxb, u64 pwb, int xcd, int lane, u64& jb, unsigned& jo) {
  n_ = n_ < 413 ? n_ : 413;
  const int b2 = BOTH && n_ >= 207, r = n_ - 207 * b2, st = (r * 2850) >> 16, p = r - 23 * st;
  const int q = (p - 8) * 64 + lane, kr = (q * 4370) >> 16, r15 = q - 15 * kr, m = (r15 * 13) >> 6, kq = r15 - 5 * m;
  const u64 px = b2 ? pxb : pxa, pw = b2 ? pwb : pwa;
  jb = (p < 8 ? px : pw) + (u64)(unsigned)(p < 8 ? ((xcd >> 1) * 512 + p * 64) * 1152 + st * 128 : st * (64 * 3456) + (xcd & 1) * 512);
  jo = (unsigned)(p < 8 ? lane * 1152 : kr * 3456 + m * 1152 + kq * 128);
}
DEV void warm1(u64 jb, unsigned jo) {
  asm volatile("s_mov_b32 m0, %2\n\ts_nop 0\n\tglobal_load_lds_dword %0, %1" ::"v"(jo), "s"(jb), "s"(L_DUMMY) : "memory", "m0");
}

template <bool ISA>
DEV void loader(const char* XB, const char* W, const char* WO, const char* X1, const char* W1, char* WS, LAS char* L, int xcd, int jx, int wid) {
  const int rt = (xcd >> 1) * 2 + (jx >> 3), hp = (xcd & 1) * 8 + (jx & 7);
  const int lw = wid - 8;
  const char* X = XB + (size_t)(rt * 256 + lw * 64) * 1152;
  const int lane = threadIdx.x & 63;
  RSTAMP(30); STAMP(0);
  __builtin_amdgcn_s_setprio(2);
  __amdgpu_buffer_rsrc_t wsr = __builtin_amdgcn_make_buffer_rsrc(WS, 0, W_TOTAL, 0x00020000);
  u64 tag = 0;
  if (ISA) { if (LDSLEEP) __builtin_amdgcn_s_sleep(LDSLEEP); }
  else {
    const u32x4 s4 = {SENT, SENT, SENT, SENT};
    const int ob = W_OB + rt * 16 * 18432, u = lw * 64 + lane;
#pragma unroll
    for (int i = 0; i < 8; ++i)
      __builtin_amdgcn_raw_buffer_store_b128(s4, wsr, ob + lw * 4 * 18432 + hp * 1024 + lane * 16, (i & 3) * 18432 + (i >> 2) * (8 * 16 * 18432), 16);
    __builtin_amdgcn_raw_buffer_store_b128(s4, wsr, ob + (u >> 7) * (8 * 16 * 18432) + ((u >> 3) & 15) * 18432 + 16384 + hp * 128 + (u & 7) * 16, 0, 16);
    __builtin_amdgcn_raw_buffer_store_b128(s4, wsr, u < 180 ? W_KV + (hp * 8 + rt) * 2880 + u * 16 : OOB, 0, 16);
    STAMP(21);
    tag = read_tag(WS);
    if (BLDSLEEP) __builtin_amdgcn_s_sleep(BLDSLEEP);
  }
  const int r8 = lane >> 3;
  const unsigned xo0 = (unsigned)((4 * (r8 >> 1) + (r8 & 1)) * 1152 + (((lane & 7) ^ (r8 >> 1)) * 16));
  const unsigned xo1 = (xo0 ^ 64u) + 16 * 1152;
  const int dW = lw * 3840, dX = 15360 + lw * 8192;
  auto opX = [&](int s, int i) {
    glds64(X + s * 128 + ((i >> 1) & 1) * 2304 + (i >> 2) * (32 * 1152), (i & 1) ? xo1 : xo0, (s % 3) * SLOT + dX + i * 1024);
  };
#pragma unroll
  for (int i = 0; i < 4; ++i) opX(0, i);
  SB();
  const int r6 = (lane * 26) >> 8, un = lane - 10 * r6;
  const int part = un >= 5, u5 = un - 5 * part;
  const int wcol = ((hp * 72) & ~15) + u5 * 16;
  int kap[4];
  unsigned wj[4];
#pragma unroll
  for (int j = 0; j < 4; ++j) {
    kap[j] = kmap(24 * lw + 6 * j + r6);
    wj[j] = (unsigned)(kap[j] < 64 ? wcol + (1 + part) * 1152 + kap[j] * 3456 : wcol + (kap[j] - 64 + 32 * part) * 3456);
  }
  auto opW = [&](int s, int j) { glds60(W + s * (64 * 3456), wj[j], (s % 3) * SLOT + dW + j * 960); };
#pragma unroll
  for (int j = 0; j < 4; ++j) opW(0, j);
#pragma unroll
  for (int i = 4; i < 8; ++i) opX(0, i);
  const int g = lane >> 4, c = lane & 15;
  const char* XH = XB + (size_t)(rt * 256 - (rt > 0 ? 16 : 0)) * 1152;
  unsigned ho = 0;
  if (!ISA) {
    const int hc = 4 * lw + (r8 & 3);
    ho = (unsigned)((rt > 0 ? (4 * ((hc & 7) >> 1) + (hc & 1) + 2 * (hc >> 3)) * 1152 : 0) + (((lane & 7) ^ (hc >> 1)) * 16));
  }
  auto opH = [&](int s) { glds32(XH + s * 128, ho, (s % 3) * SLOT + 48128 + lw * 512); };
  if (!ISA) opH(0);
  const int tq = (lane >> 2) & 3, tp = lane & 3;
  const int wrow = (16 * (g >> 1) + 4 * (g & 1) + tq) * 160 + (hp & 1) * 8;
  const int hwa = wrow + 16 * (tp & 1) + 40 * (tp >> 1) + (lw >> 1) * 80 + (lw & 1) * 8;
  const int hwx = wrow + 32 + (tp == 1 ? 80 : 0);
  const int hxa = 48128 + c * 128 + ((g ^ (c >> 1)) * 16);
  f32x4 hacc = {0.f, 0.f, 0.f, 0.f}, hacx = hacc;
  bf16x8 hw[2], hw6[2];
  u32x4 hxf[2];
  auto hread = [&](int s) {
    if ((HX & 1) && s < 8) return;
    const int o = (s % 3) * SLOT;
    hxf[0] = lds128(L, o + hxa); hxf[1] = lds128(L, (o + hxa) ^ 64);
    hw[0] = trfrag(L, o + hwa); hw[1] = trfrag(L, o + hwa + 5120);
    if (lw == 0) { hw6[0] = trfrag(L, o + hwx); hw6[1] = trfrag(L, o + hwx + 5120); }
  };
  auto hmul = [&]() {
    hacc = mfma(hw[0], asb(hxf[0]), hacc); hacc = mfma(hw[1], asb(hxf[1]), hacc);
    if (lw == 0) { hacx = mfma(hw6[0], asb(hxf[0]), hacx); hacx = mfma(hw6[1], asb(hxf[1]), hacx); }
  };
  const int rto = xcd, cg = jx & 7;
  const int wocol = cg * 144 + (un > 8 ? 8 : un) * 16;
  unsigned wo[4], wl[4];
  auto opO = [&](int n) {
    const int bk = n >> 2, j = n & 3;
    glds60(WO + (bk < 5 ? 108 * bk * 1152 : 0), bk < 5 ? wo[j] : wl[j], L_WO + bk * 15360 + dW + j * 960);
  };
  unsigned fo = 0, ajo[2];
  u64 ajb[2];
  u32x2 fv = {0u, 0u};
  sfor<8>([&](auto S) {
    constexpr int s = S + 1;
    if constexpr (s == 3) {
#pragma unroll
      for (int j = 0; j < 4; ++j) { wo[j] = (unsigned)opq(worow(kap[j]) * 1152 + wocol); wl[j] = (unsigned)opq(worow(480 + kap[j]) * 1152 + wocol); }
      if constexpr (ISA && AJ) {
#pragma unroll
        for (int i = 0; i < 2; ++i) {
          job<true>(J1 + (NPRO + i * 4 + lw) * 16 + jx, 0, 0, (u64)X1, (u64)W1, xcd, lane, ajb[i], ajo[i]);
          asm volatile("" : "+v"(ajo[i]), "+s"(ajb[i]));
        }
      }
      fo = (unsigned)opq(W_F + 1024 + (lane < 16 ? rto * 16 + lane : (lane & 7) * 16 + hp) * 8);
    }
    sfor<(ISA ? 12 : 13)>([&](auto I) {
      constexpr int i = I - (!ISA && I > HP);
      if constexpr (!ISA && I == HP) opH(s);
      else if constexpr (i % 3 < 2) opX(s, 2 * (i / 3) + i % 3); else opW(s, i / 3);
      constexpr int ld = ISA ? (s <= LEADNA ? LEAD0 : LEADA) : (s <= LEADNB ? LEAD0 : LEADB);
      if constexpr (I == ld - 1) {
        if constexpr (s < 6) { STAMP(s == 1 ? 1 : s == 2 ? 4 : s == 3 ? 7 : s == 4 ? 9 : 10); }
        if constexpr (!ISA && s > 1 && !(HX & 1)) { SB(); hmul(); SB(); }
        waitbar<ld>();
        if constexpr (!ISA) { hread(s - 1); SB(); }
        if constexpr (s < 5) { STAMP(s == 1 ? 2 : 9 + s); }
      }
    });
  });
  if constexpr (!ISA && !(HX & 1)) { SB(); hmul(); SB(); }
  waitbar<0>();
  if constexpr (!ISA) { hread(8); SB(); }
  gload64s(fv, WS, fo);
  constexpr int NWM = 2 + (ISA && AJ ? 2 : 0), NW1 = ISA ? NWO1A : NWO1B, NW2 = ISA ? NWO2A : 24;
  {
    if ((ISA ? WS0A : WS0B) > 0) __builtin_amdgcn_s_sleep(ISA ? WS0A : WS0B);
    if constexpr (ISA && AJ) { warm1(ajb[0], ajo[0]); warm1(ajb[1], ajo[1]); }
    const u64 jb = (u64)(WO + (jx * 4 + lw) * (81 * 128));
    warm1(jb, lane * 128);
    warm1(jb, (lane < 16 ? lane + 64 : 80) * 128);
    if ((ISA ? WSLA : WSLB) > 0) __builtin_amdgcn_s_sleep(ISA ? WSLA : WSLB);
  }
#pragma unroll
  for (int n = 0; n < NW1; ++n) opO(n);
  STAMP(5);
  if constexpr (!ISA) {
    SB();
    hmul();
    const int lh = c >> 3, hr = g * 144 + (c & 7);
    const int hb = L_REC + hr * 16 + (lw < 2 ? lh * 9216 + lw * 8 : lw * 9216 + lh * 2), hs = lw < 2 ? 2 : 4;
#pragma unroll
    for (int i = 0; i < 4; i += 2) {
      const unsigned v = pk2(hacc[i], hacc[i + 1]);
      *(LAS unsigned short*)(L + hb + i * hs) = (unsigned short)v;
      *(LAS unsigned short*)(L + hb + (i + 1) * hs) = (unsigned short)(v >> 16);
    }
    if (lw == 0 && g < 2) {
      const int xb = L_REC + 36864 + hr * 8 + (g ? lh * 2 : lh * 4), xs = g ? 4 : 2;
#pragma unroll
      for (int i = 0; i < 4; i += 2) {
        const unsigned v = pk2(hacx[i], hacx[i + 1]);
        *(LAS unsigned short*)(L + xb + i * 1152) = (unsigned short)v;
        *(LAS unsigned short*)(L + xb + i * 1152 + xs) = (unsigned short)(v >> 16);
      }
    }
  }
  if (ISA && lw == 3) tag = read_tag(WS);
  asm volatile("s_waitcnt vmcnt(%1)" : "+v"(fv) : "n"(NWM + NW1) : "memory");
  if (ISA && lw == 3) {
    __builtin_amdgcn_raw_buffer_store_b64(u32x2{(unsigned)tag, (unsigned)(tag >> 32)}, wsr, lane == 0 ? W_F + (rt * 16 + hp) * 8 : OOB, 0, 16);
  }
  if (lw == 3 && !__all(fv[0] == (unsigned)tag && fv[1] == (unsigned)(tag >> 32))) {
    const GAS u64* fp = (const GAS u64*)(WS + fo);
#pragma unroll 1
    for (int spin = 0; spin < SPIN_CAP; ++spin) {
      u64 v = __hip_atomic_load(fp, __ATOMIC_RELAXED, __HIP_MEMORY_SCOPE_AGENT);
      if (__all(v == tag)) break;
      __builtin_amdgcn_s_sleep(2);
    }
  }
  STAMP(6);
  bar();
  STAMP(8);
#pragma unroll
  for (int n = NW1; n < NW2; ++n) opO(n);
  if (ISA && lw < 3) {
    const int u = lw * 64 + lane;
    const int kvl = u < 180 ? W_KV + hp * 8 * 2880 + u * 16 : OOB;
    u32x4 pt[8];
    unsigned pend = 0xFFu;
    __builtin_amdgcn_s_sleep(KVSLEEP);
#pragma unroll 1
    for (int spin = 0; spin < SPIN_CAP; ++spin) {
#pragma unroll
      for (int i = 0; i < 8; ++i)
        if (pend >> i & 1) pt[i] = __builtin_amdgcn_raw_buffer_load_b128(wsr, kvl + i * 2880, 0, 16);
      unsigned np = 0;
#pragma unroll
      for (int i = 0; i < 8; ++i) {
        unsigned m = umax3(pt[i][0], pt[i][1], pt[i][2]);
        m = m > pt[i][3] ? m : pt[i][3];
        np |= __any(m == SENT) ? 1u << i : 0u;
      }
      pend = np;
      if (!pend) break;
      __builtin_amdgcn_s_sleep(1);
    }
    STAMP(14);
    float s[8];
    const unsigned selLo = (unsigned)opq(0x00003F80), selHi = (unsigned)opq(0x3F800000);
#pragma unroll
    for (int j = 0; j < 8; ++j) s[j] = 0.f;
#pragma unroll
    for (int i = 0; i < 8; ++i) {
#pragma unroll
      for (int j = 0; j < 4; ++j) {
        s[2 * j] = dot2(pt[i][j], selLo, s[2 * j]);
        s[2 * j + 1] = dot2(pt[i][j], selHi, s[2 * j + 1]);
      }
    }
    if (u < 180) *(LAS u32x4*)(L + L_KVT + u * 16) = u32x4{pk2(s[0], s[1]), pk2(s[2], s[3]), pk2(s[4], s[5]), pk2(s[6], s[7])};
  }
  if constexpr (ISA) {
    bar();
#pragma unroll
    for (int n = NW2; n < 24; ++n) opO(n);
  }
  waitv<0>();
  STAMP(15);
  bar();
}

template <bool ISA>
DEV void body(const char* XA, const char* WA, const char* XB, const char* W, const char* PHI, char* OUT, char* WS, LAS char* L, int xcd, int jx, u64& tag) {
  const int br = ISA ? 0 : 1;
  const int rt = (xcd >> 1) * 2 + (jx >> 3), hp = (xcd & 1) * 8 + (jx & 7);
  const int tid = threadIdx.x;
  const int wid = __builtin_amdgcn_readfirstlane(tid >> 6);
  int lane = tid & 63;
  int g = lane >> 4, c = lane & 15;
  RSTAMP(30); STAMP(0);

  u64 jb; unsigned jo;
  const u64 pxa = (u64)XA, pwa = (u64)WA, pxb = (u64)XB, pwb = (u64)W;
#define JOB(N) job<!ISA>(N, pxa, pwa, pxb, pwb, xcd, lane, jb, jo)
  auto warm = [&]() {
    asm volatile("s_mov_b32 m0, %2\n\ts_nop 0\n\tglobal_load_lds_dword %0, %1" ::"v"(jo), "s"(jb), "s"(L_DUMMY) : "memory", "m0");
  };
  auto warmw = [&](int w) {
    asm volatile("s_cmp_eq_u32 %3, %4\n\ts_cselect_b64 exec, -1, 0\n\ts_setprio 3\n\ts_mov_b32 m0, %2\n\ts_nop 0\n\tglobal_load_lds_dword %0, %1\n\ts_setprio 0\n\ts_mov_b64 exec, -1"
                 ::"v"(jo), "s"(jb), "s"(L_DUMMY), "s"(wid), "n"(w) : "memory", "m0", "scc");
  };
  static_assert(NT - NPRO <= 8 && NPRO <= NT && J1 + NPRO * 16 >= 207, "");
  if (wid < (ISA ? NRA : 4)) {
    JOB(wid < NRA ? wid * 32 + br * 16 + jx : NRA * 32 + (wid - NRA) * 16 + jx);
    if (wid > 0) __builtin_amdgcn_s_sleep(PFD);
    if (wid > 1) __builtin_amdgcn_s_sleep(PFD);
    if (wid > 2) __builtin_amdgcn_s_sleep(PFD);
    warm();
  }
  if (!ISA) {
    const int t1 = (wid + 4) & 7;
    __builtin_amdgcn_s_sleep(R2SLEEP);
    tag = read_tag(WS);
#pragma unroll
    for (int i = 0; i < 3; ++i)
      if (t1 + 8 * i < NPRO) { JOB(J1 + (t1 + 8 * i) * 16 + jx); warm(); }
    if (wid == 3 && jx < 3 && xcd >= 2) {
      const int st = jx * 4 + g;
      jb = pxb + (u64)(unsigned)(((xcd >> 1) * 512 - 16) * 1152);
      jo = (unsigned)(c * 1152 + (st < 8 ? st : 8) * 128);
      warm();
    }
    if (!AJ) {
      JOB(J1 + (NPRO + ((t1 - NPRO) & 7)) * 16 + jx);
      asm volatile("" : "+v"(jo), "+s"(jb));
    }
  }
  const int dX = 15360 + wid * 4096;
  __amdgpu_buffer_rsrc_t wsr = __builtin_amdgcn_make_buffer_rsrc(WS, 0, W_TOTAL, 0x00020000);
  unsigned phv = 0, phw = 0;
  __amdgpu_buffer_rsrc_t pr = __builtin_amdgcn_make_buffer_rsrc((void*)(PHI + hp * 1296), 0, 1296, 0x00020000);
  int phs = 0;
  if (ISA) {
    int st = tid >= 384, e3 = tid - 384 * st;
    int t = st ? e3 >> 5 : e3 >> 7;
    int a = st ? e3 & 31 : (e3 >> 2) & 31;
    int gg = a >> 3, f = 9 * ((a >> 2) & 1) + 4 * t + (a & 3);
    int d = st ? (gg == 0 ? 16 : 0) : (8 * (gg & 1) + 2 * (gg >> 1) + 2 * (e3 & 3));
    bool ok = (4 * t + (a & 3) < 9) && !(st && (gg & 1)) && tid < 480;
    phs = opq(ok ? (((gg >> 1) * 18 + d) * 18 + f) * 2 : OOB);
  }
  auto phload = [&]() {
    asm volatile("s_setprio 3\n\tbuffer_load_ushort %0, %2, %3, 0 offen\n\tbuffer_load_ushort %1, %2, %3, 0 offen offset:36\n\ts_setprio 0"
                 : "=&v"(phv), "=&v"(phw) : "v"(phs), "s"(pr) : "memory");
  };
  const int obt = W_OB + (br * 8 + rt) * 16 * 18432;
  int obm = obt + 2 * wid * 18432 + hp * 1024 + lane * 16;
  const int xb = W_KV + (hp * 8 + rt) * 2880;
  const int flo = tid == 448 ? W_F + ((br * 8 + rt) * 16 + hp) * 8 : OOB;
  if (ISA) phload();
  if (tid < 16) *(LAS unsigned*)(L + L_ZERO + tid * 4) = (tid & 13) == 12 ? 0x00003F80u : 0u;

  int tq = (lane >> 2) & 3, tp = lane & 3;
  int wrow = (16 * (g >> 1) + 4 * (g & 1) + tq) * 160;
  int unit = 16 * (tp & 1) + 40 * (tp >> 1);
  const int wa = wrow + unit + (hp & 1) * 8;
  const int wx0 = wrow + 32 + (hp & 1) * 8 + (tp == 1 ? 80 : tp == 2 ? 10240 : 0);
  const int wx1 = wx0 + (tp == 2 ? 80 : 5120);
  const int rto = xcd, hf = jx >> 3, cg = jx & 7;

  f32x4 acc[7][2];
#pragma unroll
  for (int t = 0; t < 7; ++t) { acc[t][0] = f32x4{0.f, 0.f, 0.f, 0.f}; acc[t][1] = acc[t][0]; }

  STAMP(1);
  bar();
  STAMP(2); RSTAMP(25);
  bf16x8 wf[2][7];
  u32x4 xf[2][2];
  int wa_s[3], wx_s[3][2], xa_s[3];
#pragma unroll
  for (int i = 0; i < 3; ++i) {
    wa_s[i] = opq(wa + i * SLOT); wx_s[i][0] = opq(wx0 + i * SLOT); wx_s[i][1] = opq(wx1 + i * SLOT);
    xa_s[i] = opq(i * SLOT + dX + c * 128 + ((g ^ (c >> 1)) * 16));
  }
  auto rdW = [&](auto KN, auto T) {
    constexpr int kn = KN, t = T, sl = (kn >> 1) % 3, od = kn & 1;
    if constexpr (t < 4) return trfrag(L, wa_s[sl] + od * 5120 + (t >> 1) * 80 + (t & 1) * 8);
    else if constexpr (t < 6) return trfrag(L, wa_s[sl] + 10240 + od * 80 + (t & 1) * 8);
    else return trfrag(L, wx_s[sl][od]);
  };
  auto rdX = [&](auto KN, int mt) {
    constexpr int kn = KN, sl = (kn >> 1) % 3, od = kn & 1;
    return lds128(L, (od ? xa_s[sl] ^ 64 : xa_s[sl]) + mt * 2048);
  };
  using I0 = std::integral_constant<int, 0>;
  xf[0][0] = rdX(I0{}, 0); xf[0][1] = rdX(I0{}, 1);
  sfor<7>([&](auto T) { wf[0][T] = rdW(I0{}, T); });
  sfor<18>([&](auto K) {
    constexpr int k = K, cur = k & 1, nxt = cur ^ 1, kn = k + 1;
    using KN = std::integral_constant<int, kn>;
    if constexpr (ISA && k == KW) waitv<0>();
    if constexpr ((k & 1) && kn < 18) bar();
    if constexpr (ISA && k == KW) {
      asm volatile("" : "+v"(phv), "+v"(phw));
      if (tid < 480) *(LAS unsigned*)(L + L_PT + tid * 4) = phv | (phw << 16);
    }
    sfor<7>([&](auto T) {
      constexpr int t = T;
      SB();
      acc[t][0] = mfma(wf[cur][t], asb(xf[cur][0]), acc[t][0]);
      if constexpr (kn < 18) {
        if constexpr (t == 0) { xf[nxt][0] = rdX(KN{}, 0); xf[nxt][1] = rdX(KN{}, 1); }
        wf[nxt][t] = rdW(KN{}, T);
      }
      SB();
      acc[t][1] = mfma(wf[cur][t], asb(xf[cur][1]), acc[t][1]);
      if constexpr (!ISA && k == 0 && t == 0) __builtin_amdgcn_raw_buffer_store_b64(u32x2{(unsigned)tag, (unsigned)(tag >> 32)}, wsr, flo, 0, 16);
      if constexpr (!ISA && !AJ && JB && t == 1 && k == J3K && NPRO < NT)
        asm volatile("s_cmp_lt_u32 %3, %4\n\ts_cselect_b64 exec, -1, 0\n\ts_mov_b32 m0, %2\n\ts_nop 0\n\tglobal_load_lds_dword %0, %1\n\ts_mov_b64 exec, -1"
                     ::"v"(jo), "s"(jb), "s"(L_DUMMY), "s"((wid + 4 - NPRO) & 7), "n"(NT - NPRO) : "memory", "m0", "scc");
      if constexpr (!ISA && !AJ && !JB && t == 1 && k >= J3K && (k - J3K) % J3S == 0 && NPRO + (k - J3K) / J3S < NT) warmw((4 + NPRO + (k - J3K) / J3S) & 7);
    });
    SB();
    if constexpr (k == 5 || k == 11) STAMP(3 + (k == 11));
  });
  STAMP(5); RSTAMP(26);
  lane = opq(tid) & 63; g = lane >> 4; c = lane & 15; tq = (lane >> 2) & 3; tp = lane & 3;
  const bool act = (c >> 3) == (g >> 1);
  const int pta = L_PT + (g * 8 + (c & 7)) * 16;
  const int ptb = L_PT + 1536 + (g * 8 + (c & 7)) * 4;

  u32x4 km[2], qm[2];
  unsigned ex[2], qx[2];
#pragma unroll
  for (int mt = 0; mt < 2; ++mt) {
    km[mt] = u32x4{pk2(acc[0][mt][0], acc[0][mt][1]), pk2(acc[0][mt][2], acc[0][mt][3]), pk2(acc[1][mt][0], acc[1][mt][1]), pk2(acc[1][mt][2], acc[1][mt][3])};
    qm[mt] = u32x4{pk2(acc[4][mt][0], acc[4][mt][1]), pk2(acc[4][mt][2], acc[4][mt][3]), pk2(acc[5][mt][0], acc[5][mt][1]), pk2(acc[5][mt][2], acc[5][mt][3])};
    ex[mt] = pk2(acc[6][mt][0], acc[6][mt][1]);
    qx[mt] = pk2(acc[6][mt][2], acc[6][mt][3]);
    if (!ISA && (g & 1)) {
      ex[mt] = pk2(acc[6][0][mt], acc[6][1][mt]);
      qx[mt] = pk2(acc[6][0][2 + mt], acc[6][1][2 + mt]);
    }
    swap32(ex[mt], qx[mt]);
    qx[mt] = (g & 1) ? 0u : qx[mt];
  }
  u32x4 om[2]; unsigned ox[2];
  if (ISA) {
    STAMP(6);
    const int rbase = L_REC + g * 12288;
    const int pc = g & 1;
#pragma unroll
    for (int mt = 0; mt < 2; ++mt) {
      const int rp = 32 * wid + 16 * mt + c;
      *(LAS u32x4*)(L + rbase + 8192 + (rp ^ (8 * pc)) * 16) =
          u32x4{pk2(acc[2][mt][0], acc[2][mt][1]), pk2(acc[2][mt][2], acc[2][mt][3]), pk2(acc[3][mt][0], acc[3][mt][1]), pk2(acc[3][mt][2], acc[3][mt][3])};
    }
    {
      unsigned ph[2][9];
      unsigned ek[2] = {pc ? 0u : ex[0], pc ? 0u : ex[1]};
      features(L, pta, ptb, act, km, ek, ph, [&](int mt) {
        const int rp = 32 * wid + 16 * mt + c;
        *(LAS u32x4*)(L + rbase + rp * 16) = u32x4{ph[mt][0], ph[mt][1], ph[mt][2], ph[mt][3]};
        *(LAS u32x4*)(L + rbase + 4096 + (rp ^ 8) * 16) = u32x4{ph[mt][4], ph[mt][5], ph[mt][6], ph[mt][7]};
        *(LAS u32x2*)(L + L_REC + 49152 + g * 2112 + rp * 8) = u32x2{ph[mt][8], ex[mt]};
      });
    }
    STAMP(7);
    bar();
    STAMP(8);
    {
      const int hd = wid & 1, nt = (wid >> 1) & 1;
      const int rowb = 16 * (g >> 1) + 4 * (g & 1) + tq;
      const int hb = L_REC + hd * 2 * 12288;
      const int sel = tp >> 1;
      const int aM0 = hb + (tp & 1) * 8 + sel * 4096 + (rowb + 8 * sel) * 16;
      const int aN0 = hb + (tp & 1) * 8 + 8192 + sel * 12288 + (rowb + 8 * sel) * 16;
      const int aS = L_REC + 49152 + (2 * hd + (tp & 1)) * 2112 + rowb * 8;
      const int a = (c >> 3) * 8 + (c & 7);
      const int b = (c >= 2 && c < 6) ? 14 + c : -1;
      const int dI = nt == 0 ? a : b;
      const int n0 = nt == 0 ? aN0 : (tp == 0 ? aS + 2112 : L_ZERO + 48);
      const int n1 = nt == 0 ? aN0 ^ 128 : (tp == 0 ? n0 + 64 : n0);
      const int nst = nt == 0 ? 512 : (tp == 0 ? 256 : 0);
      auto tr2 = [&](int p, int q) {
        s16x4 u = __builtin_amdgcn_ds_read_tr16_b64_v4i16((LAS s16x4*)(L + p));
        s16x4 v = __builtin_amdgcn_ds_read_tr16_b64_v4i16((LAS s16x4*)(L + q));
        u32x2 ua = __builtin_bit_cast(u32x2, u), ub = __builtin_bit_cast(u32x2, v);
        return asb(u32x4{ua.x, ua.y, ub.x, ub.y});
      };
      f32x4 D0 = {0.f, 0.f, 0.f, 0.f}, D1 = D0;
      bf16x8 nf[8];
#pragma unroll
      for (int ks = 0; ks < 8; ++ks) nf[ks] = tr2(n0 + ks * nst, n1 + ks * nst);
      if (wid < 4) {
        bf16x8 m0[8], m1[8];
#pragma unroll
        for (int ks = 0; ks < 8; ++ks) {
          m0[ks] = tr2(aM0 + ks * 512, (aM0 ^ 128) + ks * 512);
          m1[ks] = tr2(aM0 + 12288 + ks * 512, (aM0 ^ 128) + 12288 + ks * 512);
        }
#pragma unroll
        for (int ks = 0; ks < 8; ++ks) { D0 = mfma(m0[ks], nf[ks], D0); D1 = mfma(m1[ks], nf[ks], D1); }
        unsigned a0 = pk2(D0[0], D0[1]), a1 = pk2(D0[2], D0[3]), b0 = pk2(D1[0], D1[1]), b1 = pk2(D1[2], D1[3]);
        swap16(a0, b0); swap16(a1, b1);
        const int kvm = dI >= 0 ? xb + hd * 1440 + dI * 64 + (((g & 1) << 1) | (g >> 1)) * 16 : OOB;
        __builtin_amdgcn_raw_buffer_store_b128(canon4(u32x4{a0, a1, b0, b1}), wsr, kvm, 0, 16);
      } else {
        bf16x8 m0[8];
#pragma unroll
        for (int ks = 0; ks < 8; ++ks) m0[ks] = tr2(aS + ks * 256, aS + 64 + ks * 256);
#pragma unroll
        for (int ks = 0; ks < 8; ks += 2) { D0 = mfma(m0[ks], nf[ks], D0); D1 = mfma(m0[ks + 1], nf[ks + 1], D1); }
        unsigned t0 = pk2(D0[0] + D1[0], D0[1] + D1[1]), t1 = t0;
        swap16(t0, t1);
        const int kvx = (dI >= 0 && g == 0) ? xb + hd * 1440 + 1280 + dI * 8 : OOB;
        __builtin_amdgcn_raw_buffer_store_b64(u32x2{canon(t0), canon(t1)}, wsr, kvx, 0, 16);
      }
    }
    STAMP(9);
    unsigned ph[2][9];
    features(L, pta, ptb, act, qm, qx, ph, [&](int) {});
    STAMP(13);
    bar();
    STAMP(15);
    const int hb = L_KVT + (g >> 1) * 1440;
    f32x4 O[2][3];
#pragma unroll
    for (int t = 0; t < 3; ++t) {
      const int d = t < 2 ? ((c >> 2) & 1) * 8 + 4 * t + (c & 3) : 16 + (c & 3);
      const int a0 = act ? hb + d * 64 + (g & 1) * 32 : L_ZERO;
      const int a2 = act ? hb + 1280 + d * 8 + (g & 1) * 4 : L_ZERO;
      u32x4 T0 = lds128(L, a0), T1 = lds128(L, a0 + 16);
      u32x4 T2 = {lds32(L, a2), 0u, 0u, 0u};
#pragma unroll
      for (int mt = 0; mt < 2; ++mt) {
        f32x4 z = {0.f, 0.f, 0.f, 0.f};
        z = mfma(asb(T0), asb(u32x4{ph[mt][0], ph[mt][1], ph[mt][2], ph[mt][3]}), z);
        z = mfma(asb(T1), asb(u32x4{ph[mt][4], ph[mt][5], ph[mt][6], ph[mt][7]}), z);
        O[mt][t] = mfma(asb(T2), asb(u32x4{ph[mt][8], 0u, 0u, 0u}), z);
      }
    }
#pragma unroll
    for (int mt = 0; mt < 2; ++mt) {
      float rd = __builtin_amdgcn_rcpf(O[mt][2][2] + 1e-6f);
      om[mt] = u32x4{pk2(O[mt][0][0] * rd, O[mt][0][1] * rd), pk2(O[mt][0][2] * rd, O[mt][0][3] * rd), pk2(O[mt][1][0] * rd, O[mt][1][1] * rd), pk2(O[mt][1][2] * rd, O[mt][1][3] * rd)};
      ox[mt] = pk2(O[mt][2][0] * rd, O[mt][2][1] * rd);
    }
  } else {
    u32x4 own[4]; u32x2 ownx;
    const int rec = g * 144 + 8 + 16 * wid + c;
    own[0] = km[0]; own[1] = km[1];
    own[2] = u32x4{pk2(acc[2][0][0], acc[2][1][0]), pk2(acc[2][0][1], acc[2][1][1]), pk2(acc[2][0][2], acc[2][1][2]), pk2(acc[2][0][3], acc[2][1][3])};
    own[3] = u32x4{pk2(acc[3][0][0], acc[3][1][0]), pk2(acc[3][0][1], acc[3][1][1]), pk2(acc[3][0][2], acc[3][1][2]), pk2(acc[3][0][3], acc[3][1][3])};
    ownx = u32x2{ex[0], ex[1]};
    STAMP(6);
#pragma unroll
    for (int q = 0; q < 4; ++q) *(LAS u32x4*)(L + L_REC + q * 9216 + rec * 16) = own[q];
    *(LAS u32x2*)(L + L_REC + 36864 + rec * 8) = ownx;
    STAMP(7);
    bar();
    STAMP(8);
    STAMP(13);
    u32x4 R[5][4]; u32x2 RX[5];
#pragma unroll
    for (int q = 0; q < 4; ++q) R[0][q] = own[q];
    RX[0] = ownx;
#pragma unroll
    for (int j = 1; j < 5; ++j) {
#pragma unroll
      for (int q = 0; q < 4; ++q) if (!(j == 4 && q == 0)) R[j][q] = lds128(L, L_REC + q * 9216 + (rec - 2 * j) * 16);
      RX[j] = lds64(L, L_REC + 36864 + (rec - 2 * j) * 8);
    }
    auto qk = [&](int mt, int j, int lh) {
      float a = dot2(qx[mt], RX[j][lh], 0.f);
#pragma unroll
      for (int d = 0; d < 4; ++d) a = dot2(qm[mt][d], R[j][lh][d], a);
      return a;
    };
    float lg[8];
#pragma unroll
    for (int sl = 0; sl < 8; ++sl) {
      float a = sl == 0 ? qk(0, 4, 1) : qk(0, sl >> 1, (sl & 1) ^ 1);
      float b = sl == 0 ? qk(1, 0, 1) : qk(1, sl >> 1, (sl & 1) ^ 1);
      swap16f(a, b);
      lg[sl] = (a + b) * 0.23570226039551584f;
    }
    if (rt == 0 && wid == 0) {
      const int i = c >> 1;
#pragma unroll
      for (int sl = 0; sl < 8; ++sl) {
        int j = sl == 0 ? ((g & 1) ? 0 : 4) : (sl >> 1);
        lg[sl] = i < j ? -1e9f : lg[sl];
      }
    }
    float mx = lg[0];
#pragma unroll
    for (int sl = 1; sl < 8; ++sl) mx = __builtin_fmaxf(mx, lg[sl]);
    const float LOG2E = 1.4426950408889634f;
    float sum = 0.f;
#pragma unroll
    for (int sl = 0; sl < 8; ++sl) { lg[sl] = __builtin_amdgcn_exp2f((lg[sl] - mx) * LOG2E); sum += lg[sl]; }
    float rs = __builtin_amdgcn_rcpf(sum);
    unsigned ul[5], uh[4];
#pragma unroll
    for (int j = 0; j < 4; ++j) {
      unsigned a = pk2(lg[2 * j + 1] * rs, lg[2 * j] * rs), b = a;
      swap16(a, b);
      ul[j] = a; uh[j] = b;
    }
    ul[4] = ul[0] & 0xFFFF0000u; ul[0] &= 0x0000FFFFu;
    float ol[10], oh[10];
#pragma unroll
    for (int d = 0; d < 10; ++d) {
      float a = 0.f, b = 0.f;
#pragma unroll
      for (int j = 0; j < 5; ++j) {
        unsigned v = d < 8 ? R[j][2 + (d >> 2)][d & 3] : RX[j][d - 8];
        a = dot2(ul[j], v, a);
        if (j < 4) b = dot2(uh[j], v, b);
      }
      ol[d] = a; oh[d] = b;
    }
    om[0] = u32x4{pk2(ol[0], ol[1]), pk2(ol[2], ol[3]), pk2(ol[4], ol[5]), pk2(ol[6], ol[7])};
    om[1] = u32x4{pk2(oh[0], oh[1]), pk2(oh[2], oh[3]), pk2(oh[4], oh[5]), pk2(oh[6], oh[7])};
    ox[0] = pk2(ol[8], ol[9]); ox[1] = pk2(oh[8], oh[9]);
  }
  STAMP(16); RSTAMP(27);
  lane = opq(tid) & 63; g = lane >> 4; c = lane & 15; tq = (lane >> 2) & 3; tp = lane & 3;
  obm = obt + 2 * wid * 18432 + hp * 1024 + lane * 16;
  wrow = (16 * (g >> 1) + 4 * (g & 1) + tq) * 160;
  unit = 16 * (tp & 1) + 40 * (tp >> 1);
  {
    const int obr = g == 1 ? obt + 2 * wid * 18432 + 16384 + hp * 128 + c * 8 : OOB;
#pragma unroll
    for (int mt = 0; mt < 2; ++mt) __builtin_amdgcn_raw_buffer_store_b128(canon4(om[mt]), wsr, obm + mt * 18432, 0, 16);
#pragma unroll
    for (int mt = 0; mt < 2; ++mt) {
      unsigned t0 = ox[mt], t1 = t0;
      swap32(t0, t1);
      __builtin_amdgcn_raw_buffer_store_b64(u32x2{canon(t0), canon(t1)}, wsr, obr + mt * 18432, 0, 16);
    }
  }
  STAMP(17);

  {
    const int tile = hf * 8 + wid;
    const int ob = W_OB + ((br * 8 + rto) * 16 + tile) * 18432;
    u32x4 of[18];
    const int oa_m = ob + lane * 16, oa_r = ob + 16384 + 2 * g * 128 + c * 8;
    auto ldL = [&]() {
#pragma unroll
      for (int i = 0; i < 2; ++i) {
        u32x2 a = __builtin_amdgcn_raw_buffer_load_b64(wsr, oa_r + i * 1024, 0, 16);
        u32x2 b = __builtin_amdgcn_raw_buffer_load_b64(wsr, oa_r + i * 1024 + 128, 0, 16);
        of[16 + i] = u32x4{a[0], a[1], b[0], b[1]};
      }
    };
    auto bad = [&](int i) {
      unsigned m = umax3(of[i][0], of[i][1], of[i][2]);
      m = m > of[i][3] ? m : of[i][3];
      return __any(m == SENT);
    };
    const int oa = wrow + unit + L_WO;
    const int os = wrow + ((tp & 1) ? 13 : 4) * 8 + L_WO;
    f32x4 C[5];
    bf16x8 wg[3][5];
    if ((ISA ? CSLA : CSLB) > 0) __builtin_amdgcn_s_sleep(ISA ? CSLA : CSLB);
    ldL();
    bar();
#pragma unroll 1
    for (int spin = 0; spin < SPIN_CAP; ++spin) {
#pragma unroll
      for (int i = 0; i < GD; ++i) of[i] = __builtin_amdgcn_raw_buffer_load_b128(wsr, oa_m, i * 1024, 16);
      SB();
#pragma unroll
      for (int t = 0; t < 5; ++t) C[t] = f32x4{0.f, 0.f, 0.f, 0.f};
      const int oa_l = opq(oa), os_l = opq(os), oa_h = opq(oa + 46080), os_h = opq(os + 46080);
      auto rdO = [&](auto J, auto T) {
        constexpr int kk = (J + 16) % 18, t = T, no = (kk % 9) * 5120;
        if constexpr (t < 4) return trfrag(L, (kk < 9 ? oa_l : oa_h) + no + (t >> 1) * 72 + (t & 1) * 8);
        else return trfrag(L, (kk < 9 ? os_l : os_h) + no);
      };
      sfor<5>([&](auto T) { wg[0][T] = rdO(I0{}, T); });
      sfor<5>([&](auto T) { wg[1][T] = rdO(std::integral_constant<int, 1>{}, T); });
      SB();
      if (bad(16) | bad(17)) {
#pragma unroll 1
        for (int sp2 = 0; sp2 < SPIN_CAP; ++sp2) {
          __builtin_amdgcn_s_sleep(GSLEEP);
          ldL();
          if (!(bad(16) | bad(17))) break;
        }
#pragma unroll
        for (int i = 0; i < GD; ++i)
          if (bad(i)) of[i] = __builtin_amdgcn_raw_buffer_load_b128(wsr, oa_m, i * 1024, 16);
        __builtin_amdgcn_s_waitcnt(0x0F70);
      }
      STAMP(18); RSTAMP(28);
      unsigned m = 0;
      sfor<18>([&](auto J) {
        constexpr int j = J, cur = j % 3, nxt = (j + 2) % 3, kk = (j + 16) % 18;
        sfor<5>([&](auto T) {
          SB();
          C[T] = mfma(wg[cur][T], asb(of[kk]), C[T]);
          if constexpr (j < 16) wg[nxt][T] = rdO(std::integral_constant<int, j + 2>{}, T);
          if constexpr (T == 0 && GD + j < 16) of[GD + j] = __builtin_amdgcn_raw_buffer_load_b128(wsr, oa_m, (GD + j) * 1024, 16);
          if constexpr (T == 1 && kk < 16) m = umax3(m, of[kk][0], of[kk][1]);
          if constexpr (T == 2 && kk < 16) m = umax3(m, of[kk][2], of[kk][3]);
        });
        SB();
      });
      if (__all(m != SENT)) break;
    }
    STAMP(19); RSTAMP(29);
    const int row = rto * 256 + 32 * (tile >> 1) + 4 * (c >> 1) + (c & 1) + 2 * (tile & 1);
    GAS char* o = (GAS char*)OUT + (size_t)row * 2304 + br * 1152 + cg * 144;
    const int cu = 16 * (g & 1) + 40 * (g >> 1);
    typedef unsigned u32x4a __attribute__((ext_vector_type(4), aligned(8)));
    *(GAS u32x4a*)(o + cu) = u32x4{pk2(C[0][0], C[0][1]), pk2(C[0][2], C[0][3]), pk2(C[1][0], C[1][1]), pk2(C[1][2], C[1][3])};
    *(GAS u32x4a*)(o + cu + 72) = u32x4{pk2(C[2][0], C[2][1]), pk2(C[2][2], C[2][3]), pk2(C[3][0], C[3][1]), pk2(C[3][2], C[3][3])};
    if (g < 2) *(GAS u32x2*)(o + (g ? 13 : 4) * 8) = u32x2{pk2(C[4][0], C[4][1]), pk2(C[4][2], C[4][3])};
  }
}

__global__ __launch_bounds__(768, 3) void fused(Params P) {
  extern __shared__ __attribute__((aligned(16))) char lds_raw[];
  LAS char* L = (LAS char*)lds_raw;
  const int b = blockIdx.x, xcd = b & 7, br = b >> 7, jx = (b >> 3) & 15;
  const char* XA = P.x[0];
  const char* WA = P.w[0];
  const char* X = br ? P.x[1] : P.x[0];
  const char* W = br ? P.w[1] : P.w[0];
  const char* WO = br ? P.wo[1] : P.wo[0];
  const char* X1 = P.x[1];
  const char* W1 = P.w[1];
  const char* PHI = P.wphi;
  char* OUT = P.out;
  char* WS = P.ws;
  asm volatile("" : "+s"(X), "+s"(W), "+s"(WO), "+s"(PHI), "+s"(OUT), "+s"(WS), "+s"(XA), "+s"(WA), "+s"(X1), "+s"(W1));
  u64 tag = 0;
  const int wid = __builtin_amdgcn_readfirstlane(threadIdx.x >> 6);
  if (wid >= 8) {
    if (br == 0) loader<true>(X, W, WO, X1, W1, WS, L, xcd, jx, wid);
    else loader<false>(X, W, WO, X1, W1, WS, L, xcd, jx, wid);
    return;
  }
  if (br == 0) body<true>(X, W, X, W, PHI, OUT, WS, L, xcd, jx, tag);
  else body<false>(XA, WA, X, W, PHI, OUT, WS, L, xcd, jx, tag);
  if (b == 255 && threadIdx.x < 64) {
    const GAS u64* f = (const GAS u64*)(WS + W_F) + threadIdx.x;
#pragma unroll 1
    for (int spin = 0; spin < SPIN_CAP; ++spin) {
      bool e = true;
#pragma unroll
      for (int i = 0; i < 4; ++i) e &= __hip_atomic_load(f + 64 * i, __ATOMIC_RELAXED, __HIP_MEMORY_SCOPE_AGENT) == tag;
      if (__all(e)) break;
      __builtin_amdgcn_s_sleep(2);
    }
    if (threadIdx.x == 0) __hip_atomic_store((GAS u64*)(WS + W_EPOCH), tag, __ATOMIC_RELAXED, __HIP_MEMORY_SCOPE_AGENT);
  }
}

extern "C" void kernel_launch(void* const* d_in, const int* in_sizes, int n_in, void* d_out, int out_size, void* d_ws,
                              size_t ws_size, hipStream_t stream) {
  Params P;
  P.x[0] = (const char*)d_in[0]; P.w[0] = (const char*)d_in[1]; P.wphi = (const char*)d_in[2]; P.wo[0] = (const char*)d_in[3];
  P.x[1] = (const char*)d_in[4]; P.w[1] = (const char*)d_in[5]; P.wo[1] = (const char*)d_in[6];
  P.out = (char*)d_out; P.ws = (char*)d_ws;
  (void)hipFuncSetAttribute((const void*)fused, hipFuncAttributeMaxDynamicSharedMemorySize, LDS_BYTES);
  hipLaunchKernelGGL(fused, dim3(256), dim3(768), LDS_BYTES, stream, P);
}
```

```cpp
#include <hip/hip_runtime.h>
#include <stdint.h>
#include <type_traits>
#pragma clang diagnostic ignored "-Winline-asm"

typedef short s16x4 __attribute__((ext_vector_type(4)));
typedef __bf16 bf16x8 __attribute__((ext_vector_type(8)));
typedef __bf16 bf16x2 __attribute__((ext_vector_type(2)));
typedef float f32x4 __attribute__((ext_vector_type(4)));
typedef float f32x2 __attribute__((ext_vector_type(2)));
typedef unsigned u32x4 __attribute__((ext_vector_type(4)));
typedef unsigned u32x2 __attribute__((ext_vector_type(2)));
typedef unsigned long long u64;
#define LAS __attribute__((address_space(3)))
#define GAS __attribute__((address_space(1)))
#define DEV __device__ __forceinline__
#define SB() __builtin_amdgcn_sched_barrier(0)

constexpr int SLOT = 50176;
constexpr int L_REC = 0;
constexpr int L_WO = 57600;
constexpr int L_ZERO = 3 * SLOT;
constexpr int L_PT = L_ZERO + 64;
constexpr int L_KVT = L_PT + 1920;
constexpr int L_DUMMY = L_KVT + 2880;
constexpr int L_END = L_DUMMY + 256;
static_assert(L_END + 3072 <= 162816 && L_WO + 92160 <= L_ZERO && L_WO + 30720 <= 2 * SLOT, "lds");
constexpr int LDS_BYTES = 163840;

constexpr int W_EPOCH = 0;
constexpr int W_F = 4096;
constexpr int W_KV = 65536;
constexpr int W_OB = 1048576;
constexpr int W_TOTAL = W_OB + 2 * 8 * 16 * 18432;
constexpr unsigned SENT = 0xFFFFFFFFu;
constexpr int OOB = 0x40000000;
constexpr int SPIN_CAP = 400000;
#define KVSLEEP 24
#define LDSLEEP 4
#define BLDSLEEP 30
#define BTH 0
#define BTN 4
#define NPRO 12
#define R2SLEEP 20
#define NRA 2
#define PFD 4
#define J3K 0
#define J3S 1
#define KW 3
#define LEAD0 3
#define LEADNA 2
#define LEADNB 2
#define LEADA 12
#define LEADB 12
#define HP 12
#define NWO1A 0
#define NWO1B 0
#define WSLA 0
#define WSLB 0
#define WS0A 0
#define WS0B 0
#define JB 0
#define CSLA 28
#define CSLB 24
#define ACN 0
#define ACK 12
#define CPF 3
#define OAUX 0
#define GD 4
#define HX 0
#define AJ 1
#define NWO2A 12
#define GSLEEP 2

template <int N> DEV void waitv() { asm volatile("s_waitcnt vmcnt(%0)" ::"n"(N) : "memory"); }
DEV void bar() { asm volatile("s_waitcnt lgkmcnt(0)\n\ts_barrier" ::: "memory"); }
template <int N> DEV void waitbar() { asm volatile("s_waitcnt vmcnt(%0)\n\ts_barrier" ::"n"(N) : "memory"); }

DEV unsigned pk2(float a, float b) {
  f32x2 v = {a, b};
  bf16x2 r = __builtin_convertvector(v, bf16x2);
  return __builtin_bit_cast(unsigned, r);
}
DEV bf16x8 asb(u32x4 v) { return __builtin_bit_cast(bf16x8, v); }
DEV f32x4 mfma(bf16x8 a, bf16x8 b, f32x4 c) { return __builtin_amdgcn_mfma_f32_16x16x32_bf16(a, b, c, 0, 0, 0); }
DEV float dot2(unsigned a, unsigned b, float c) {
  return __builtin_amdgcn_fdot2_f32_bf16(__builtin_bit_cast(bf16x2, a), __builtin_bit_cast(bf16x2, b), c, false);
}
DEV u32x4 lds128(LAS char* L, int off) { return *(LAS u32x4*)(L + off); }
DEV u32x2 lds64(LAS char* L, int off) { return *(LAS u32x2*)(L + off); }
DEV unsigned lds32(LAS char* L, int off) { return *(LAS unsigned*)(L + off); }
DEV bf16x8 trfrag(LAS char* L, int off) {
  s16x4 a = __builtin_amdgcn_ds_read_tr16_b64_v4i16((LAS s16x4*)(L + off));
  s16x4 b = __builtin_amdgcn_ds_read_tr16_b64_v4i16((LAS s16x4*)(L + off + 1280));
  u32x2 ua = __builtin_bit_cast(u32x2, a), ub = __builtin_bit_cast(u32x2, b);
  u32x4 r = {ua.x, ua.y, ub.x, ub.y};
  return asb(r);
}
DEV void glds60(const char* base, unsigned voff, int dst) {
  asm volatile("s_mov_b32 m0, %2\n\ts_lshr_b64 exec, -1, 4\n\tglobal_load_lds_dwordx4 %0, %1\n\ts_mov_b64 exec, -1"
               ::"v"(voff), "s"(base), "s"(dst) : "memory", "m0", "scc");
}
DEV void glds64(const char* base, unsigned voff, int dst) {
  asm volatile("s_mov_b32 m0, %2\n\ts_nop 0\n\tglobal_load_lds_dwordx4 %0, %1" ::"v"(voff), "s"(base), "s"(dst) : "memory", "m0");
}
DEV void glds32(const char* base, unsigned voff, int dst) {
  asm volatile("s_mov_b32 m0, %2\n\ts_lshr_b64 exec, -1, 32\n\tglobal_load_lds_dwordx4 %0, %1\n\ts_mov_b64 exec, -1"
               ::"v"(voff), "s"(base), "s"(dst) : "memory", "m0", "scc");
}
template <int OFF> DEV void gload128(u32x4& d, const char* base, unsigned voff) {
  asm volatile("global_load_dwordx4 %0, %1, %2 offset:%3" : "=v"(d) : "v"(voff), "s"(base), "n"(OFF) : "memory");
}
DEV void gload64s(u32x2& d, const char* base, unsigned voff) {
  asm volatile("global_load_dwordx2 %0, %1, %2 sc1" : "=v"(d) : "v"(voff), "s"(base) : "memory");
}
#define STAMP(i) do {} while (0)
#define RSTAMP(i) do {} while (0)
DEV int opq(int v) { asm volatile("" : "+v"(v)); return v; }
DEV void swap16(unsigned& a, unsigned& b) {
  auto r = __builtin_amdgcn_permlane16_swap(a, b, false, false);
  a = r[0]; b = r[1];
}
DEV void swap32(unsigned& a, unsigned& b) {
  auto r = __builtin_amdgcn_permlane32_swap(a, b, false, false);
  a = r[0]; b = r[1];
}
DEV void swap16f(float& a, float& b) {
  unsigned x = __float_as_uint(a), y = __float_as_uint(b);
  swap16(x, y);
  a = __uint_as_float(x); b = __uint_as_float(y);
}
DEV unsigned canon(unsigned x) { return x < 0xFFFFFFFEu ? x : 0xFFFFFFFEu; }
DEV unsigned umax3(unsigned a, unsigned b, unsigned c) { unsigned m = a > b ? a : b; return m > c ? m : c; }
DEV u32x4 canon4(u32x4 v) { return u32x4{canon(v[0]), canon(v[1]), canon(v[2]), canon(v[3])}; }

struct Params {
  const char* x[2];
  const char* w[2];
  const char* wo[2];
  const char* wphi;
  char* out;
  char* ws;
};

DEV int kmap(int p) { return (p & ~12) | ((p & 4) << 1) | ((p & 8) >> 1); }
DEV int worow(int k) {
  int m = k + 4 * (((k >> 4) + 1) >> 1);
  int kr = k - 512;
  int r = 36 * (kr >> 2) + 16 + (kr & 3);
  return k < 512 ? m : r;
}

template <class F>
DEV void features(LAS char* L, int pta, int ptb, bool act, const u32x4 (&m)[2], const unsigned (&e)[2], unsigned (&ph)[2][9], F&& done) {
  f32x4 P[2][3];
#pragma unroll
  for (int t = 0; t < 3; ++t) {
    u32x4 T0 = lds128(L, act ? pta + t * 512 : L_ZERO);
    u32x4 T1 = {lds32(L, act ? ptb + t * 128 : L_ZERO), 0u, 0u, 0u};
#pragma unroll
    for (int mt = 0; mt < 2; ++mt) {
      f32x4 z = {0.f, 0.f, 0.f, 0.f};
      u32x4 E = {e[mt], 0u, 0u, 0u};
      z = mfma(asb(T0), asb(m[mt]), z);
      P[mt][t] = mfma(asb(T1), asb(E), z);
    }
  }
  const float LOG2E = 1.4426950408889634f;
  float p[2][9], mx[2], mn[2];
#pragma unroll
  for (int mt = 0; mt < 2; ++mt) {
#pragma unroll
    for (int i = 0; i < 9; ++i) p[mt][i] = P[mt][i >> 2][i & 3];
    float a = p[mt][0], b = p[mt][0];
#pragma unroll
    for (int i = 1; i < 9; ++i) { a = __builtin_fmaxf(a, p[mt][i]); b = __builtin_fminf(b, p[mt][i]); }
    float a2 = a, b2 = b;
    swap16f(a, a2); swap16f(b, b2);
    mx[mt] = __builtin_fmaxf(a, a2) * LOG2E; mn[mt] = __builtin_fminf(b, b2) * LOG2E;
  }
#pragma unroll
  for (int mt = 0; mt < 2; ++mt) {
    float ep[9], en[9], sp = 0.f, sn = 0.f;
#pragma unroll
    for (int i = 0; i < 9; ++i) {
      ep[i] = __builtin_amdgcn_exp2f(__builtin_fmaf(p[mt][i], LOG2E, -mx[mt]));
      en[i] = __builtin_amdgcn_exp2f(__builtin_fmaf(p[mt][i], -LOG2E, mn[mt]));
      sp += ep[i]; sn += en[i];
    }
    float sp2 = sp, sn2 = sn;
    swap16f(sp, sp2); swap16f(sn, sn2);
    float rp = __builtin_amdgcn_rcpf(sp + sp2), rn = __builtin_amdgcn_rcpf(sn + sn2);
    float f[18];
#pragma unroll
    for (int i = 0; i < 9; ++i) { f[i] = ep[i] * rp; f[9 + i] = en[i] * rn; }
#pragma unroll
    for (int i = 0; i < 9; ++i) ph[mt][i] = pk2(f[2 * i], f[2 * i + 1]);
    done(mt);
  }
}

template <int N, class F> DEV void sfor(F&& f) {
  if constexpr (N > 0) { sfor<N - 1>(f); f(std::integral_constant<int, N - 1>{}); }
}

DEV u64 next_tag(u64 t);
DEV u64 read_tag(const char* ws) {
  u64 t;
  asm volatile("s_load_dwordx2 %0, %1, 0x0\n\ts_waitcnt lgkmcnt(0)" : "=s"(t) : "s"(ws) : "memory");
  return next_tag(t);
}
DEV u64 next_tag(u64 t) { return (t ^ 0x9E3779B97F4A7C15ull) * 0xD1342543DE82EF95ull + 0x632BE59BD9B4E019ull; }

constexpr int J1 = 64 + 16 * NRA, NT = (414 - J1 + 15) / 16;
template <bool BOTH>
DEV void job(int n_, u64 pxa, u64 pwa, u64 pxb, u64 pwb, int xcd, int lane, u64& jb, unsigned& jo) {
  n_ = n_ < 413 ? n_ : 413;
  const int b2 = BOTH && n_ >= 207, r = n_ - 207 * b2, st = (r * 2850) >> 16, p = r - 23 * st;
  const int q = (p - 8) * 64 + lane, kr = (q * 4370) >> 16, r15 = q - 15 * kr, m = (r15 * 13) >> 6, kq = r15 - 5 * m;
  const u64 px = b2 ? pxb : pxa, pw = b2 ? pwb : pwa;
  jb = (p < 8 ? px : pw) + (u64)(unsigned)(p < 8 ? ((xcd >> 1) * 512 + p * 64) * 1152 + st * 128 : st * (64 * 3456) + (xcd & 1) * 512);
  jo = (unsigned)(p < 8 ? lane * 1152 : kr * 3456 + m * 1152 + kq * 128);
}
DEV void warm1(u64 jb, unsigned jo) {
  asm volatile("s_mov_b32 m0, %2\n\ts_nop 0\n\tglobal_load_lds_dword %0, %1" ::"v"(jo), "s"(jb), "s"(L_DUMMY) : "memory", "m0");
}

template <bool ISA>
DEV void loader(const char* XB, const char* W, const char* WO, const char* X1, const char* W1, char* WS, LAS char* L, int xcd, int jx, int wid) {
  const int rt = (xcd >> 1) * 2 + (jx >> 3), hp = (xcd & 1) * 8 + (jx & 7);
  const int lw = wid - 8;
  const char* X = XB + (size_t)(rt * 256 + lw * 64) * 1152;
  const int lane = threadIdx.x & 63;
  RSTAMP(30); STAMP(0);
  __builtin_amdgcn_s_setprio(2);
  __amdgpu_buffer_rsrc_t wsr = __builtin_amdgcn_make_buffer_rsrc(WS, 0, W_TOTAL, 0x00020000);
  u64 tag = 0;
  if (ISA) { if (LDSLEEP) __builtin_amdgcn_s_sleep(LDSLEEP); }
  else {
    const u32x4 s4 = {SENT, SENT, SENT, SENT};
    const int ob = W_OB + rt * 16 * 18432, u = lw * 64 + lane;
#pragma unroll
    for (int i = 0; i < 8; ++i)
      __builtin_amdgcn_raw_buffer_store_b128(s4, wsr, ob + lw * 4 * 18432 + hp * 1024 + lane * 16, (i & 3) * 18432 + (i >> 2) * (8 * 16 * 18432), 16);
    __builtin_amdgcn_raw_buffer_store_b128(s4, wsr, ob + (u >> 7) * (8 * 16 * 18432) + ((u >> 3) & 15) * 18432 + 16384 + hp * 128 + (u & 7) * 16, 0, 16);
    __builtin_amdgcn_raw_buffer_store_b128(s4, wsr, u < 180 ? W_KV + (hp * 8 + rt) * 2880 + u * 16 : OOB, 0, 16);
    STAMP(21);
    tag = read_tag(WS);
    if (BLDSLEEP) __builtin_amdgcn_s_sleep(BLDSLEEP);
  }
  const int r8 = lane >> 3;
  const unsigned xo0 = (unsigned)((4 * (r8 >> 1) + (r8 & 1)) * 1152 + (((lane & 7) ^ (r8 >> 1)) * 16));
  const unsigned xo1 = (xo0 ^ 64u) + 16 * 1152;
  const int dW = lw * 3840, dX = 15360 + lw * 8192;
  auto opX = [&](int s, int i) {
    glds64(X + s * 128 + ((i >> 1) & 1) * 2304 + (i >> 2) * (32 * 1152), (i & 1) ? xo1 : xo0, (s % 3) * SLOT + dX + i * 1024);
  };
#pragma unroll
  for (int i = 0; i < 4; ++i) opX(0, i);
  SB();
  const int r6 = (lane * 26) >> 8, un = lane - 10 * r6;
  const int part = un >= 5, u5 = un - 5 * part;
  const int wcol = ((hp * 72) & ~15) + u5 * 16;
  int kap[4];
  unsigned wj[4];
#pragma unroll
  for (int j = 0; j < 4; ++j) {
    kap[j] = kmap(24 * lw + 6 * j + r6);
    wj[j] = (unsigned)(kap[j] < 64 ? wcol + (1 + part) * 1152 + kap[j] * 3456 : wcol + (kap[j] - 64 + 32 * part) * 3456);
  }
  auto opW = [&](int s, int j) { glds60(W + s * (64 * 3456), wj[j], (s % 3) * SLOT + dW + j * 960); };
#pragma unroll
  for (int j = 0; j < 4; ++j) opW(0, j);
#pragma unroll
  for (int i = 4; i < 8; ++i) opX(0, i);
  const int g = lane >> 4, c = lane & 15;
  const char* XH = XB + (size_t)(rt * 256 - (rt > 0 ? 16 : 0)) * 1152;
  unsigned ho = 0;
  if (!ISA) {
    const int hc = 4 * lw + (r8 & 3);
    ho = (unsigned)((rt > 0 ? (4 * ((hc & 7) >> 1) + (hc & 1) + 2 * (hc >> 3)) * 1152 : 0) + (((lane & 7) ^ (hc >> 1)) * 16));
  }
  auto opH = [&](int s) { glds32(XH + s * 128, ho, (s % 3) * SLOT + 48128 + lw * 512); };
  if (!ISA) opH(0);
  const int tq = (lane >> 2) & 3, tp = lane & 3;
  const int wrow = (16 * (g >> 1) + 4 * (g & 1) + tq) * 160 + (hp & 1) * 8;
  const int hwa = wrow + 16 * (tp & 1) + 40 * (tp >> 1) + (lw >> 1) * 80 + (lw & 1) * 8;
  const int hwx = wrow + 32 + (tp == 1 ? 80 : 0);
  const int hxa = 48128 + c * 128 + ((g ^ (c >> 1)) * 16);
  f32x4 hacc = {0.f, 0.f, 0.f, 0.f}, hacx = hacc;
  bf16x8 hw[2], hw6[2];
  u32x4 hxf[2];
  auto hread = [&](int s) {
    if ((HX & 1) && s < 8) return;
    const int o = (s % 3) * SLOT;
    hxf[0] = lds128(L, o + hxa); hxf[1] = lds128(L, (o + hxa) ^ 64);
    hw[0] = trfrag(L, o + hwa); hw[1] = trfrag(L, o + hwa + 5120);
    if (lw == 0) { hw6[0] = trfrag(L, o + hwx); hw6[1] = trfrag(L, o + hwx + 5120); }
  };
  auto hmul = [&]() {
    hacc = mfma(hw[0], asb(hxf[0]), hacc); hacc = mfma(hw[1], asb(hxf[1]), hacc);
    if (lw == 0) { hacx = mfma(hw6[0], asb(hxf[0]), hacx); hacx = mfma(hw6[1], asb(hxf[1]), hacx); }
  };
  const int rto = xcd, cg = jx & 7;
  const int wocol = cg * 144 + (un > 8 ? 8 : un) * 16;
  unsigned wo[4], wl[4];
  auto opO = [&](int n) {
    const int bk = n >> 2, j = n & 3;
    glds60(WO + (bk < 5 ? 108 * bk * 1152 : 0), bk < 5 ? wo[j] : wl[j], L_WO + bk * 15360 + dW + j * 960);
  };
  unsigned fo = 0, ajo[2];
  u64 ajb[2];
  u32x2 fv = {0u, 0u};
  sfor<8>([&](auto S) {
    constexpr int s = S + 1;
    if constexpr (s == 3) {
#pragma unroll
      for (int j = 0; j < 4; ++j) { wo[j] = (unsigned)opq(worow(kap[j]) * 1152 + wocol); wl[j] = (unsigned)opq(worow(480 + kap[j]) * 1152 + wocol); }
      if constexpr (ISA && AJ) {
#pragma unroll
        for (int i = ACN / 4; i < 2; ++i) {
          job<true>(J1 + ((NT - 8 > NPRO ? NT - 8 : NPRO) + i * 4 + lw) * 16 + jx, 0, 0, (u64)X1, (u64)W1, xcd, lane, ajb[i], ajo[i]);
          asm volatile("" : "+v"(ajo[i]), "+s"(ajb[i]));
        }
      }
      fo = (unsigned)opq(W_F + 1024 + (lane < 16 ? rto * 16 + lane : (lane & 7) * 16 + hp) * 8);
    }
    sfor<(ISA ? 12 : 13)>([&](auto I) {
      constexpr int i = I - (!ISA && I > HP);
      if constexpr (!ISA && I == HP) opH(s);
      else if constexpr (i % 3 < 2) opX(s, 2 * (i / 3) + i % 3); else opW(s, i / 3);
      constexpr int ld = ISA ? (s <= LEADNA ? LEAD0 : LEADA) : (s <= LEADNB ? LEAD0 : LEADB);
      if constexpr (I == ld - 1) {
        if constexpr (s < 6) { STAMP(s == 1 ? 1 : s == 2 ? 4 : s == 3 ? 7 : s == 4 ? 9 : 10); }
        if constexpr (!ISA && s > 1 && !(HX & 1)) { SB(); hmul(); SB(); }
        waitbar<ld>();
        if constexpr (!ISA) { hread(s - 1); SB(); }
        if constexpr (s < 5) { STAMP(s == 1 ? 2 : 9 + s); }
      }
    });
  });
  if constexpr (!ISA && !(HX & 1)) { SB(); hmul(); SB(); }
  waitbar<0>();
  if constexpr (!ISA) { hread(8); SB(); }
  gload64s(fv, WS, fo);
  constexpr int NWM = 2 + (ISA && AJ ? 2 - ACN / 4 : 0), NW1 = ISA ? NWO1A : NWO1B, NW2 = ISA ? NWO2A : 24;
  {
    if ((ISA ? WS0A : WS0B) > 0) __builtin_amdgcn_s_sleep(ISA ? WS0A : WS0B);
    if constexpr (ISA && AJ && ACN < 4) warm1(ajb[0], ajo[0]);
    if constexpr (ISA && AJ && ACN < 8) warm1(ajb[1], ajo[1]);
    const u64 jb = (u64)(WO + (jx * 4 + lw) * (81 * 128));
    warm1(jb, lane * 128);
    warm1(jb, (lane < 16 ? lane + 64 : 80) * 128);
    if ((ISA ? WSLA : WSLB) > 0) __builtin_amdgcn_s_sleep(ISA ? WSLA : WSLB);
  }
#pragma unroll
  for (int n = 0; n < NW1; ++n) opO(n);
  STAMP(5);
  if constexpr (!ISA) {
    SB();
    hmul();
    const int lh = c >> 3, hr = g * 144 + (c & 7);
    const int hb = L_REC + hr * 16 + (lw < 2 ? lh * 9216 + lw * 8 : lw * 9216 + lh * 2), hs = lw < 2 ? 2 : 4;
#pragma unroll
    for (int i = 0; i < 4; i += 2) {
      const unsigned v = pk2(hacc[i], hacc[i + 1]);
      *(LAS unsigned short*)(L + hb + i * hs) = (unsigned short)v;
      *(LAS unsigned short*)(L + hb + (i + 1) * hs) = (unsigned short)(v >> 16);
    }
    if (lw == 0 && g < 2) {
      const int xb = L_REC + 36864 + hr * 8 + (g ? lh * 2 : lh * 4), xs = g ? 4 : 2;
#pragma unroll
      for (int i = 0; i < 4; i += 2) {
        const unsigned v = pk2(hacx[i], hacx[i + 1]);
        *(LAS unsigned short*)(L + xb + i * 1152) = (unsigned short)v;
        *(LAS unsigned short*)(L + xb + i * 1152 + xs) = (unsigned short)(v >> 16);
      }
    }
  }
  if (ISA && lw == 3) tag = read_tag(WS);
  asm volatile("s_waitcnt vmcnt(%1)" : "+v"(fv) : "n"(NWM + NW1) : "memory");
  if (ISA && lw == 3) {
    __builtin_amdgcn_raw_buffer_store_b64(u32x2{(unsigned)tag, (unsigned)(tag >> 32)}, wsr, lane == 0 ? W_F + (rt * 16 + hp) * 8 : OOB, 0, 16);
  }
  if (lw == 3 && !__all(fv[0] == (unsigned)tag && fv[1] == (unsigned)(tag >> 32))) {
    const GAS u64* fp = (const GAS u64*)(WS + fo);
#pragma unroll 1
    for (int spin = 0; spin < SPIN_CAP; ++spin) {
      u64 v = __hip_atomic_load(fp, __ATOMIC_RELAXED, __HIP_MEMORY_SCOPE_AGENT);
      if (__all(v == tag)) break;
      __builtin_amdgcn_s_sleep(2);
    }
  }
  STAMP(6);
  bar();
  STAMP(8);
#pragma unroll
  for (int n = NW1; n < NW2; ++n) opO(n);
  if (ISA && lw < 3) {
    const int u = lw * 64 + lane;
    const int kvl = u < 180 ? W_KV + hp * 8 * 2880 + u * 16 : OOB;
    u32x4 pt[8];
    unsigned pend = 0xFFu;
    __builtin_amdgcn_s_sleep(KVSLEEP);
#pragma unroll 1
    for (int spin = 0; spin < SPIN_CAP; ++spin) {
#pragma unroll
      for (int i = 0; i < 8; ++i)
        if (pend >> i & 1) pt[i] = __builtin_amdgcn_raw_buffer_load_b128(wsr, kvl + i * 2880, 0, 16);
      unsigned np = 0;
#pragma unroll
      for (int i = 0; i < 8; ++i) {
        unsigned m = umax3(pt[i][0], pt[i][1], pt[i][2]);
        m = m > pt[i][3] ? m : pt[i][3];
        np |= __any(m == SENT) ? 1u << i : 0u;
      }
      pend = np;
      if (!pend) break;
      __builtin_amdgcn_s_sleep(1);
    }
    STAMP(14);
    float s[8];
    const unsigned selLo = (unsigned)opq(0x00003F80), selHi = (unsigned)opq(0x3F800000);
#pragma unroll
    for (int j = 0; j < 8; ++j) s[j] = 0.f;
#pragma unroll
    for (int i = 0; i < 8; ++i) {
#pragma unroll
      for (int j = 0; j < 4; ++j) {
        s[2 * j] = dot2(pt[i][j], selLo, s[2 * j]);
        s[2 * j + 1] = dot2(pt[i][j], selHi, s[2 * j + 1]);
      }
    }
    if (u < 180) *(LAS u32x4*)(L + L_KVT + u * 16) = u32x4{pk2(s[0], s[1]), pk2(s[2], s[3]), pk2(s[4], s[5]), pk2(s[6], s[7])};
  }
  if constexpr (ISA) {
    bar();
#pragma unroll
    for (int n = NW2; n < 24; ++n) opO(n);
  }
  waitv<0>();
  STAMP(15);
  bar();
}

template <bool ISA>
DEV void body(const char* XA, const char* WA, const char* XB, const char* W, const char* PHI, char* OUT, char* WS, LAS char* L, int xcd, int jx, u64& tag) {
  const int br = ISA ? 0 : 1;
  const int rt = (xcd >> 1) * 2 + (jx >> 3), hp = (xcd & 1) * 8 + (jx & 7);
  const int tid = threadIdx.x;
  const int wid = __builtin_amdgcn_readfirstlane(tid >> 6);
  int lane = tid & 63;
  int g = lane >> 4, c = lane & 15;
  RSTAMP(30); STAMP(0);

  u64 jb; unsigned jo;
  const u64 pxa = (u64)XA, pwa = (u64)WA, pxb = (u64)XB, pwb = (u64)W;
#define JOB(N) job<!ISA>(N, pxa, pwa, pxb, pwb, xcd, lane, jb, jo)
  auto warm = [&]() {
    asm volatile("s_mov_b32 m0, %2\n\ts_nop 0\n\tglobal_load_lds_dword %0, %1" ::"v"(jo), "s"(jb), "s"(L_DUMMY) : "memory", "m0");
  };
  auto warmw = [&](int w) {
    asm volatile("s_cmp_eq_u32 %3, %4\n\ts_cselect_b64 exec, -1, 0\n\ts_setprio 3\n\ts_mov_b32 m0, %2\n\ts_nop 0\n\tglobal_load_lds_dword %0, %1\n\ts_setprio 0\n\ts_mov_b64 exec, -1"
                 ::"v"(jo), "s"(jb), "s"(L_DUMMY), "s"(wid), "n"(w) : "memory", "m0", "scc");
  };
  static_assert((AJ || NT - NPRO <= 8) && NPRO <= NT && J1 + NPRO * 16 >= 207, "");
  if (wid < (ISA ? NRA : 4)) {
    JOB(wid < NRA ? wid * 32 + br * 16 + jx : NRA * 32 + (wid - NRA) * 16 + jx);
    if (wid > 0) __builtin_amdgcn_s_sleep(PFD);
    if (wid > 1) __builtin_amdgcn_s_sleep(PFD);
    if (wid > 2) __builtin_amdgcn_s_sleep(PFD);
    warm();
  }
  if (!ISA) {
    const int t1 = (wid + 4) & 7;
    __builtin_amdgcn_s_sleep(R2SLEEP);
    tag = read_tag(WS);
#pragma unroll
    for (int i = 0; i < 3; ++i)
      if (t1 + 8 * i < NPRO) { JOB(J1 + (t1 + 8 * i) * 16 + jx); warm(); }
    if (wid == 3 && jx < 3 && xcd >= 2) {
      const int st = jx * 4 + g;
      jb = pxb + (u64)(unsigned)(((xcd >> 1) * 512 - 16) * 1152);
      jo = (unsigned)(c * 1152 + (st < 8 ? st : 8) * 128);
      warm();
    }
    if (!AJ) {
      JOB(J1 + (NPRO + ((t1 - NPRO) & 7)) * 16 + jx);
      asm volatile("" : "+v"(jo), "+s"(jb));
    }
  }
  if (ISA && ACN > 0) {
    job<true>(J1 + (NPRO + wid) * 16 + jx, pxa, pwa, pxb, pwb, xcd, lane, jb, jo);
    asm volatile("" : "+v"(jo), "+s"(jb));
  }
  const int dX = 15360 + wid * 4096;
  __amdgpu_buffer_rsrc_t wsr = __builtin_amdgcn_make_buffer_rsrc(WS, 0, W_TOTAL, 0x00020000);
  unsigned phv = 0, phw = 0;
  __amdgpu_buffer_rsrc_t pr = __builtin_amdgcn_make_buffer_rsrc((void*)(PHI + hp * 1296), 0, 1296, 0x00020000);
  int phs = 0;
  if (ISA) {
    int st = tid >= 384, e3 = tid - 384 * st;
    int t = st ? e3 >> 5 : e3 >> 7;
    int a = st ? e3 & 31 : (e3 >> 2) & 31;
    int gg = a >> 3, f = 9 * ((a >> 2) & 1) + 4 * t + (a & 3);
    int d = st ? (gg == 0 ? 16 : 0) : (8 * (gg & 1) + 2 * (gg >> 1) + 2 * (e3 & 3));
    bool ok = (4 * t + (a & 3) < 9) && !(st && (gg & 1)) && tid < 480;
    phs = opq(ok ? (((gg >> 1) * 18 + d) * 18 + f) * 2 : OOB);
  }
  auto phload = [&]() {
    asm volatile("s_setprio 3\n\tbuffer_load_ushort %0, %2, %3, 0 offen\n\tbuffer_load_ushort %1, %2, %3, 0 offen offset:36\n\ts_setprio 0"
                 : "=&v"(phv), "=&v"(phw) : "v"(phs), "s"(pr) : "memory");
  };
  const int obt = W_OB + (br * 8 + rt) * 16 * 18432;
  int obm = obt + 2 * wid * 18432 + hp * 1024 + lane * 16;
  const int xb = W_KV + (hp * 8 + rt) * 2880;
  const int flo = tid == 448 ? W_F + ((br * 8 + rt) * 16 + hp) * 8 : OOB;
  if (ISA) phload();
  if (tid < 16) *(LAS unsigned*)(L + L_ZERO + tid * 4) = (tid & 13) == 12 ? 0x00003F80u : 0u;

  int tq = (lane >> 2) & 3, tp = lane & 3;
  int wrow = (16 * (g >> 1) + 4 * (g & 1) + tq) * 160;
  int unit = 16 * (tp & 1) + 40 * (tp >> 1);
  const int wa = wrow + unit + (hp & 1) * 8;
  const int wx0 = wrow + 32 + (hp & 1) * 8 + (tp == 1 ? 80 : tp == 2 ? 10240 : 0);
  const int wx1 = wx0 + (tp == 2 ? 80 : 5120);
  const int rto = xcd, hf = jx >> 3, cg = jx & 7;

  f32x4 acc[7][2];
#pragma unroll
  for (int t = 0; t < 7; ++t) { acc[t][0] = f32x4{0.f, 0.f, 0.f, 0.f}; acc[t][1] = acc[t][0]; }

  STAMP(1);
  bar();
  STAMP(2); RSTAMP(25);
  bf16x8 wf[2][7];
  u32x4 xf[2][2];
  int wa_s[3], wx_s[3][2], xa_s[3];
#pragma unroll
  for (int i = 0; i < 3; ++i) {
    wa_s[i] = opq(wa + i * SLOT); wx_s[i][0] = opq(wx0 + i * SLOT); wx_s[i][1] = opq(wx1 + i * SLOT);
    xa_s[i] = opq(i * SLOT + dX + c * 128 + ((g ^ (c >> 1)) * 16));
  }
  auto rdW = [&](auto KN, auto T) {
    constexpr int kn = KN, t = T, sl = (kn >> 1) % 3, od = kn & 1;
    if constexpr (t < 4) return trfrag(L, wa_s[sl] + od * 5120 + (t >> 1) * 80 + (t & 1) * 8);
    else if constexpr (t < 6) return trfrag(L, wa_s[sl] + 10240 + od * 80 + (t & 1) * 8);
    else return trfrag(L, wx_s[sl][od]);
  };
  auto rdX = [&](auto KN, int mt) {
    constexpr int kn = KN, sl = (kn >> 1) % 3, od = kn & 1;
    return lds128(L, (od ? xa_s[sl] ^ 64 : xa_s[sl]) + mt * 2048);
  };
  using I0 = std::integral_constant<int, 0>;
  xf[0][0] = rdX(I0{}, 0); xf[0][1] = rdX(I0{}, 1);
  sfor<7>([&](auto T) { wf[0][T] = rdW(I0{}, T); });
  sfor<18>([&](auto K) {
    constexpr int k = K, cur = k & 1, nxt = cur ^ 1, kn = k + 1;
    using KN = std::integral_constant<int, kn>;
    if constexpr (ISA && k == KW) waitv<0>();
    if constexpr ((k & 1) && kn < 18) bar();
    if constexpr (ISA && k == KW) {
      asm volatile("" : "+v"(phv), "+v"(phw));
      if (tid < 480) *(LAS unsigned*)(L + L_PT + tid * 4) = phv | (phw << 16);
    }
    sfor<7>([&](auto T) {
      constexpr int t = T;
      SB();
      acc[t][0] = mfma(wf[cur][t], asb(xf[cur][0]), acc[t][0]);
      if constexpr (kn < 18) {
        if constexpr (t == 0) { xf[nxt][0] = rdX(KN{}, 0); xf[nxt][1] = rdX(KN{}, 1); }
        wf[nxt][t] = rdW(KN{}, T);
      }
      SB();
      acc[t][1] = mfma(wf[cur][t], asb(xf[cur][1]), acc[t][1]);
      if constexpr (!ISA && k == 0 && t == 0) __builtin_amdgcn_raw_buffer_store_b64(u32x2{(unsigned)tag, (unsigned)(tag >> 32)}, wsr, flo, 0, 16);
      if constexpr (ISA && ACN > 0 && t == 1 && k == ACK)
        asm volatile("s_cmp_lt_u32 %3, %4\n\ts_cselect_b64 exec, -1, 0\n\ts_mov_b32 m0, %2\n\ts_nop 0\n\tglobal_load_lds_dword %0, %1\n\ts_mov_b64 exec, -1"
                     ::"v"(jo), "s"(jb), "s"(L_DUMMY), "s"(wid), "n"(ACN) : "memory", "m0", "scc");
      if constexpr (!ISA && !AJ && JB && t == 1 && k == J3K && NPRO < NT)
        asm volatile("s_cmp_lt_u32 %3, %4\n\ts_cselect_b64 exec, -1, 0\n\ts_mov_b32 m0, %2\n\ts_nop 0\n\tglobal_load_lds_dword %0, %1\n\ts_mov_b64 exec, -1"
                     ::"v"(jo), "s"(jb), "s"(L_DUMMY), "s"((wid + 4 - NPRO) & 7), "n"(NT - NPRO) : "memory", "m0", "scc");
      if constexpr (!ISA && !AJ && !JB && t == 1 && k >= J3K && (k - J3K) % J3S == 0 && NPRO + (k - J3K) / J3S < NT) warmw((4 + NPRO + (k - J3K) / J3S) & 7);
    });
    SB();
    if constexpr (k == 5 || k == 11) STAMP(3 + (k == 11));
  });
  STAMP(5); RSTAMP(26);
  lane = opq(tid) & 63; g = lane >> 4; c = lane & 15; tq = (lane >> 2) & 3; tp = lane & 3;
  const bool act = (c >> 3) == (g >> 1);
  const int pta = L_PT + (g * 8 + (c & 7)) * 16;
  const int ptb = L_PT + 1536 + (g * 8 + (c & 7)) * 4;

  u32x4 km[2], qm[2];
  unsigned ex[2], qx[2];
#pragma unroll
  for (int mt = 0; mt < 2; ++mt) {
    km[mt] = u32x4{pk2(acc[0][mt][0], acc[0][mt][1]), pk2(acc[0][mt][2], acc[0][mt][3]), pk2(acc[1][mt][0], acc[1][mt][1]), pk2(acc[1][mt][2], acc[1][mt][3])};
    qm[mt] = u32x4{pk2(acc[4][mt][0], acc[4][mt][1]), pk2(acc[4][mt][2], acc[4][mt][3]), pk2(acc[5][mt][0], acc[5][mt][1]), pk2(acc[5][mt][2], acc[5][mt][3])};
    ex[mt] = pk2(acc[6][mt][0], acc[6][mt][1]);
    qx[mt] = pk2(acc[6][mt][2], acc[6][mt][3]);
    if (!ISA && (g & 1)) {
      ex[mt] = pk2(acc[6][0][mt], acc[6][1][mt]);
      qx[mt] = pk2(acc[6][0][2 + mt], acc[6][1][2 + mt]);
    }
    swap32(ex[mt], qx[mt]);
    qx[mt] = (g & 1) ? 0u : qx[mt];
  }
  u32x4 om[2]; unsigned ox[2];
  if (ISA) {
    STAMP(6);
    const int rbase = L_REC + g * 12288;
    const int pc = g & 1;
#pragma unroll
    for (int mt = 0; mt < 2; ++mt) {
      const int rp = 32 * wid + 16 * mt + c;
      *(LAS u32x4*)(L + rbase + 8192 + (rp ^ (8 * pc)) * 16) =
          u32x4{pk2(acc[2][mt][0], acc[2][mt][1]), pk2(acc[2][mt][2], acc[2][mt][3]), pk2(acc[3][mt][0], acc[3][mt][1]), pk2(acc[3][mt][2], acc[3][mt][3])};
    }
    {
      unsigned ph[2][9];
      unsigned ek[2] = {pc ? 0u : ex[0], pc ? 0u : ex[1]};
      features(L, pta, ptb, act, km, ek, ph, [&](int mt) {
        const int rp = 32 * wid + 16 * mt + c;
        *(LAS u32x4*)(L + rbase + rp * 16) = u32x4{ph[mt][0], ph[mt][1], ph[mt][2], ph[mt][3]};
        *(LAS u32x4*)(L + rbase + 4096 + (rp ^ 8) * 16) = u32x4{ph[mt][4], ph[mt][5], ph[mt][6], ph[mt][7]};
        *(LAS u32x2*)(L + L_REC + 49152 + g * 2112 + rp * 8) = u32x2{ph[mt][8], ex[mt]};
      });
    }
    STAMP(7);
    bar();
    STAMP(8);
    {
      const int hd = wid & 1, nt = (wid >> 1) & 1;
      const int rowb = 16 * (g >> 1) + 4 * (g & 1) + tq;
      const int hb = L_REC + hd * 2 * 12288;
      const int sel = tp >> 1;
      const int aM0 = hb + (tp & 1) * 8 + sel * 4096 + (rowb + 8 * sel) * 16;
      const int aN0 = hb + (tp & 1) * 8 + 8192 + sel * 12288 + (rowb + 8 * sel) * 16;
      const int aS = L_REC + 49152 + (2 * hd + (tp & 1)) * 2112 + rowb * 8;
      const int a = (c >> 3) * 8 + (c & 7);
      const int b = (c >= 2 && c < 6) ? 14 + c : -1;
      const int dI = nt == 0 ? a : b;
      const int n0 = nt == 0 ? aN0 : (tp == 0 ? aS + 2112 : L_ZERO + 48);
      const int n1 = nt == 0 ? aN0 ^ 128 : (tp == 0 ? n0 + 64 : n0);
      const int nst = nt == 0 ? 512 : (tp == 0 ? 256 : 0);
      auto tr2 = [&](int p, int q) {
        s16x4 u = __builtin_amdgcn_ds_read_tr16_b64_v4i16((LAS s16x4*)(L + p));
        s16x4 v = __builtin_amdgcn_ds_read_tr16_b64_v4i16((LAS s16x4*)(L + q));
        u32x2 ua = __builtin_bit_cast(u32x2, u), ub = __builtin_bit_cast(u32x2, v);
        return asb(u32x4{ua.x, ua.y, ub.x, ub.y});
      };
      f32x4 D0 = {0.f, 0.f, 0.f, 0.f}, D1 = D0;
      bf16x8 nf[8];
#pragma unroll
      for (int ks = 0; ks < 8; ++ks) nf[ks] = tr2(n0 + ks * nst, n1 + ks * nst);
      if (wid < 4) {
        bf16x8 m0[8], m1[8];
#pragma unroll
        for (int ks = 0; ks < 8; ++ks) {
          m0[ks] = tr2(aM0 + ks * 512, (aM0 ^ 128) + ks * 512);
          m1[ks] = tr2(aM0 + 12288 + ks * 512, (aM0 ^ 128) + 12288 + ks * 512);
        }
#pragma unroll
        for (int ks = 0; ks < 8; ++ks) { D0 = mfma(m0[ks], nf[ks], D0); D1 = mfma(m1[ks], nf[ks], D1); }
        unsigned a0 = pk2(D0[0], D0[1]), a1 = pk2(D0[2], D0[3]), b0 = pk2(D1[0], D1[1]), b1 = pk2(D1[2], D1[3]);
        swap16(a0, b0); swap16(a1, b1);
        const int kvm = dI >= 0 ? xb + hd * 1440 + dI * 64 + (((g & 1) << 1) | (g >> 1)) * 16 : OOB;
        __builtin_amdgcn_raw_buffer_store_b128(canon4(u32x4{a0, a1, b0, b1}), wsr, kvm, 0, 16);
      } else {
        bf16x8 m0[8];
#pragma unroll
        for (int ks = 0; ks < 8; ++ks) m0[ks] = tr2(aS + ks * 256, aS + 64 + ks * 256);
#pragma unroll
        for (int ks = 0; ks < 8; ks += 2) { D0 = mfma(m0[ks], nf[ks], D0); D1 = mfma(m0[ks + 1], nf[ks + 1], D1); }
        unsigned t0 = pk2(D0[0] + D1[0], D0[1] + D1[1]), t1 = t0;
        swap16(t0, t1);
        const int kvx = (dI >= 0 && g == 0) ? xb + hd * 1440 + 1280 + dI * 8 : OOB;
        __builtin_amdgcn_raw_buffer_store_b64(u32x2{canon(t0), canon(t1)}, wsr, kvx, 0, 16);
      }
    }
    STAMP(9);
    unsigned ph[2][9];
    features(L, pta, ptb, act, qm, qx, ph, [&](int) {});
    STAMP(13);
    bar();
    STAMP(15);
    const int hb = L_KVT + (g >> 1) * 1440;
    f32x4 O[2][3];
#pragma unroll
    for (int t = 0; t < 3; ++t) {
      const int d = t < 2 ? ((c >> 2) & 1) * 8 + 4 * t + (c & 3) : 16 + (c & 3);
      const int a0 = act ? hb + d * 64 + (g & 1) * 32 : L_ZERO;
      const int a2 = act ? hb + 1280 + d * 8 + (g & 1) * 4 : L_ZERO;
      u32x4 T0 = lds128(L, a0), T1 = lds128(L, a0 + 16);
      u32x4 T2 = {lds32(L, a2), 0u, 0u, 0u};
#pragma unroll
      for (int mt = 0; mt < 2; ++mt) {
        f32x4 z = {0.f, 0.f, 0.f, 0.f};
        z = mfma(asb(T0), asb(u32x4{ph[mt][0], ph[mt][1], ph[mt][2], ph[mt][3]}), z);
        z = mfma(asb(T1), asb(u32x4{ph[mt][4], ph[mt][5], ph[mt][6], ph[mt][7]}), z);
        O[mt][t] = mfma(asb(T2), asb(u32x4{ph[mt][8], 0u, 0u, 0u}), z);
      }
    }
#pragma unroll
    for (int mt = 0; mt < 2; ++mt) {
      float rd = __builtin_amdgcn_rcpf(O[mt][2][2] + 1e-6f);
      om[mt] = u32x4{pk2(O[mt][0][0] * rd, O[mt][0][1] * rd), pk2(O[mt][0][2] * rd, O[mt][0][3] * rd), pk2(O[mt][1][0] * rd, O[mt][1][1] * rd), pk2(O[mt][1][2] * rd, O[mt][1][3] * rd)};
      ox[mt] = pk2(O[mt][2][0] * rd, O[mt][2][1] * rd);
    }
  } else {
    u32x4 own[4]; u32x2 ownx;
    const int rec = g * 144 + 8 + 16 * wid + c;
    own[0] = km[0]; own[1] = km[1];
    own[2] = u32x4{pk2(acc[2][0][0], acc[2][1][0]), pk2(acc[2][0][1], acc[2][1][1]), pk2(acc[2][0][2], acc[2][1][2]), pk2(acc[2][0][3], acc[2][1][3])};
    own[3] = u32x4{pk2(acc[3][0][0], acc[3][1][0]), pk2(acc[3][0][1], acc[3][1][1]), pk2(acc[3][0][2], acc[3][1][2]), pk2(acc[3][0][3], acc[3][1][3])};
    ownx = u32x2{ex[0], ex[1]};
    STAMP(6);
#pragma unroll
    for (int q = 0; q < 4; ++q) *(LAS u32x4*)(L + L_REC + q * 9216 + rec * 16) = own[q];
    *(LAS u32x2*)(L + L_REC + 36864 + rec * 8) = ownx;
    STAMP(7);
    bar();
    STAMP(8);
    STAMP(13);
    u32x4 R[5][4]; u32x2 RX[5];
#pragma unroll
    for (int q = 0; q < 4; ++q) R[0][q] = own[q];
    RX[0] = ownx;
#pragma unroll
    for (int j = 1; j < 5; ++j) {
#pragma unroll
      for (int q = 0; q < 4; ++q) if (!(j == 4 && q == 0)) R[j][q] = lds128(L, L_REC + q * 9216 + (rec - 2 * j) * 16);
      RX[j] = lds64(L, L_REC + 36864 + (rec - 2 * j) * 8);
    }
    auto qk = [&](int mt, int j, int lh) {
      float a = dot2(qx[mt], RX[j][lh], 0.f);
#pragma unroll
      for (int d = 0; d < 4; ++d) a = dot2(qm[mt][d], R[j][lh][d], a);
      return a;
    };
    float lg[8];
#pragma unroll
    for (int sl = 0; sl < 8; ++sl) {
      float a = sl == 0 ? qk(0, 4, 1) : qk(0, sl >> 1, (sl & 1) ^ 1);
      float b = sl == 0 ? qk(1, 0, 1) : qk(1, sl >> 1, (sl & 1) ^ 1);
      swap16f(a, b);
      lg[sl] = (a + b) * 0.23570226039551584f;
    }
    if (rt == 0 && wid == 0) {
      const int i = c >> 1;
#pragma unroll
      for (int sl = 0; sl < 8; ++sl) {
        int j = sl == 0 ? ((g & 1) ? 0 : 4) : (sl >> 1);
        lg[sl] = i < j ? -1e9f : lg[sl];
      }
    }
    float mx = lg[0];
#pragma unroll
    for (int sl = 1; sl < 8; ++sl) mx = __builtin_fmaxf(mx, lg[sl]);
    const float LOG2E = 1.4426950408889634f;
    float sum = 0.f;
#pragma unroll
    for (int sl = 0; sl < 8; ++sl) { lg[sl] = __builtin_amdgcn_exp2f((lg[sl] - mx) * LOG2E); sum += lg[sl]; }
    float rs = __builtin_amdgcn_rcpf(sum);
    unsigned ul[5], uh[4];
#pragma unroll
    for (int j = 0; j < 4; ++j) {
      unsigned a = pk2(lg[2 * j + 1] * rs, lg[2 * j] * rs), b = a;
      swap16(a, b);
      ul[j] = a; uh[j] = b;
    }
    ul[4] = ul[0] & 0xFFFF0000u; ul[0] &= 0x0000FFFFu;
    float ol[10], oh[10];
#pragma unroll
    for (int d = 0; d < 10; ++d) {
      float a = 0.f, b = 0.f;
#pragma unroll
      for (int j = 0; j < 5; ++j) {
        unsigned v = d < 8 ? R[j][2 + (d >> 2)][d & 3] : RX[j][d - 8];
        a = dot2(ul[j], v, a);
        if (j < 4) b = dot2(uh[j], v, b);
      }
      ol[d] = a; oh[d] = b;
    }
    om[0] = u32x4{pk2(ol[0], ol[1]), pk2(ol[2], ol[3]), pk2(ol[4], ol[5]), pk2(ol[6], ol[7])};
    om[1] = u32x4{pk2(oh[0], oh[1]), pk2(oh[2], oh[3]), pk2(oh[4], oh[5]), pk2(oh[6], oh[7])};
    ox[0] = pk2(ol[8], ol[9]); ox[1] = pk2(oh[8], oh[9]);
  }
  STAMP(16); RSTAMP(27);
  lane = opq(tid) & 63; g = lane >> 4; c = lane & 15; tq = (lane >> 2) & 3; tp = lane & 3;
  obm = obt + 2 * wid * 18432 + hp * 1024 + lane * 16;
  wrow = (16 * (g >> 1) + 4 * (g & 1) + tq) * 160;
  unit = 16 * (tp & 1) + 40 * (tp >> 1);
  {
    const int obr = g == 1 ? obt + 2 * wid * 18432 + 16384 + hp * 128 + c * 8 : OOB;
#pragma unroll
    for (int mt = 0; mt < 2; ++mt) __builtin_amdgcn_raw_buffer_store_b128(canon4(om[mt]), wsr, obm + mt * 18432, 0, 16);
#pragma unroll
    for (int mt = 0; mt < 2; ++mt) {
      unsigned t0 = ox[mt], t1 = t0;
      swap32(t0, t1);
      __builtin_amdgcn_raw_buffer_store_b64(u32x2{canon(t0), canon(t1)}, wsr, obr + mt * 18432, 0, 16);
    }
  }
  STAMP(17);

  {
    const int tile = hf * 8 + wid;
    const int ob = W_OB + ((br * 8 + rto) * 16 + tile) * 18432;
    u32x4 of[18];
    const int oa_m = ob + lane * 16, oa_r = ob + 16384 + 2 * g * 128 + c * 8;
    auto ldL = [&]() {
#pragma unroll
      for (int i = 0; i < 2; ++i) {
        u32x2 a = __builtin_amdgcn_raw_buffer_load_b64(wsr, oa_r + i * 1024, 0, 16);
        u32x2 b = __builtin_amdgcn_raw_buffer_load_b64(wsr, oa_r + i * 1024 + 128, 0, 16);
        of[16 + i] = u32x4{a[0], a[1], b[0], b[1]};
      }
    };
    auto bad = [&](int i) {
      unsigned m = umax3(of[i][0], of[i][1], of[i][2]);
      m = m > of[i][3] ? m : of[i][3];
      return __any(m == SENT);
    };
    const int oa = wrow + unit + L_WO;
    const int os = wrow + ((tp & 1) ? 13 : 4) * 8 + L_WO;
    f32x4 C[5];
    bf16x8 wg[3][5];
    if ((ISA ? CSLA : CSLB) > 0) __builtin_amdgcn_s_sleep(ISA ? CSLA : CSLB);
    ldL();
    bar();
#pragma unroll 1
    for (int spin = 0; spin < SPIN_CAP; ++spin) {
#pragma unroll
      for (int i = 0; i < GD; ++i) of[i] = __builtin_amdgcn_raw_buffer_load_b128(wsr, oa_m, i * 1024, 16);
      SB();
#pragma unroll
      for (int t = 0; t < 5; ++t) C[t] = f32x4{0.f, 0.f, 0.f, 0.f};
      const int oa_l = opq(oa), os_l = opq(os), oa_h = opq(oa + 46080), os_h = opq(os + 46080);
      auto rdO = [&](auto J, auto T) {
        constexpr int kk = (J + 16) % 18, t = T, no = (kk % 9) * 5120;
        if constexpr (t < 4) return trfrag(L, (kk < 9 ? oa_l : oa_h) + no + (t >> 1) * 72 + (t & 1) * 8);
        else return trfrag(L, (kk < 9 ? os_l : os_h) + no);
      };
      sfor<5>([&](auto T) { wg[0][T] = rdO(I0{}, T); });
      sfor<5>([&](auto T) { wg[1][T] = rdO(std::integral_constant<int, 1>{}, T); });
      SB();
      if (bad(16) | bad(17)) {
#pragma unroll 1
        for (int sp2 = 0; sp2 < SPIN_CAP; ++sp2) {
          __builtin_amdgcn_s_sleep(GSLEEP);
          ldL();
          if (!(bad(16) | bad(17))) break;
        }
#pragma unroll
        for (int i = 0; i < GD; ++i)
          if (bad(i)) of[i] = __builtin_amdgcn_raw_buffer_load_b128(wsr, oa_m, i * 1024, 16);
        __builtin_amdgcn_s_waitcnt(0x0F70);
      }
      STAMP(18); RSTAMP(28);
      unsigned m = 0;
      sfor<18>([&](auto J) {
        constexpr int j = J, cur = j % 3, nxt = (j + 2) % 3, kk = (j + 16) % 18;
        sfor<5>([&](auto T) {
          SB();
          C[T] = mfma(wg[cur][T], asb(of[kk]), C[T]);
          if constexpr (j < 16) wg[nxt][T] = rdO(std::integral_constant<int, j + 2>{}, T);
          if constexpr (T == 0 && GD + j < 16) of[GD + j] = __builtin_amdgcn_raw_buffer_load_b128(wsr, oa_m, (GD + j) * 1024, 16);
          if constexpr (T == 1 && kk < 16) m = umax3(m, of[kk][0], of[kk][1]);
          if constexpr (T == 2 && kk < 16) m = umax3(m, of[kk][2], of[kk][3]);
        });
        SB();
      });
      if (__all(m != SENT)) break;
    }
    STAMP(19); RSTAMP(29);
    const int row = rto * 256 + 32 * (tile >> 1) + 4 * (c >> 1) + (c & 1) + 2 * (tile & 1);
    __amdgpu_buffer_rsrc_t orr = __builtin_amdgcn_make_buffer_rsrc(OUT, 0, 2048 * 2304, 0x00020000);
    const int o = row * 2304 + br * 1152 + cg * 144;
    const int cu = 16 * (g & 1) + 40 * (g >> 1);
    __builtin_amdgcn_raw_buffer_store_b128(u32x4{pk2(C[0][0], C[0][1]), pk2(C[0][2], C[0][3]), pk2(C[1][0], C[1][1]), pk2(C[1][2], C[1][3])}, orr, o + cu, 0, OAUX);
    __builtin_amdgcn_raw_buffer_store_b128(u32x4{pk2(C[2][0], C[2][1]), pk2(C[2][2], C[2][3]), pk2(C[3][0], C[3][1]), pk2(C[3][2], C[3][3])}, orr, o + cu + 72, 0, OAUX);
    __builtin_amdgcn_raw_buffer_store_b64(u32x2{pk2(C[4][0], C[4][1]), pk2(C[4][2], C[4][3])}, orr, g < 2 ? o + (g ? 13 : 4) * 8 : OOB, 0, OAUX);
  }
}

__global__ __launch_bounds__(768, 3) void fused(Params P) {
  extern __shared__ __attribute__((aligned(16))) char lds_raw[];
  LAS char* L = (LAS char*)lds_raw;
  const int b = blockIdx.x, xcd = b & 7, br = b >> 7, jx = (b >> 3) & 15;
  u64 pc;
  asm volatile("s_getpc_b64 %0" : "=s"(pc));
  if (CPF >= 3) {
    const unsigned ix = (b >> 3) * 12 + (threadIdx.x >> 6);
    const unsigned iy = CPF == 3 ? ix : 351 - ix;
    warm1(pc & ~(u64)255, (iy < 351u ? iy : 351u) * 128);
    SB();
  }
  const char* XA = P.x[0];
  const char* WA = P.w[0];
  const char* X = br ? P.x[1] : P.x[0];
  const char* W = br ? P.w[1] : P.w[0];
  const char* WO = br ? P.wo[1] : P.wo[0];
  const char* X1 = P.x[1];
  const char* W1 = P.w[1];
  const char* PHI = P.wphi;
  char* OUT = P.out;
  char* WS = P.ws;
  asm volatile("" : "+s"(X), "+s"(W), "+s"(WO), "+s"(PHI), "+s"(OUT), "+s"(WS), "+s"(XA), "+s"(WA), "+s"(X1), "+s"(W1));
  u64 tag = 0;
  const int wid = __builtin_amdgcn_readfirstlane(threadIdx.x >> 6);
  if (wid >= 8) {
    if (br == 0) loader<true>(X, W, WO, X1, W1, WS, L, xcd, jx, wid);
    else loader<false>(X, W, WO, X1, W1, WS, L, xcd, jx, wid);
    return;
  }
  if (CPF && CPF < 3 && (CPF == 2 || br) && wid == 7) {
    const int ln = threadIdx.x & 63;
    if (CPF == 2) warm1(pc & ~(u64)255, (unsigned)(((br * 16 + jx) * 11 + (ln < 10 ? ln : 10)) * 128));
    else warm1(pc & ~(u64)255, (unsigned)((jx * 22 + (ln < 21 ? ln : 21)) * 128));
  }
  if (br == 0) body<true>(X, W, X1, W1, PHI, OUT, WS, L, xcd, jx, tag);
  else body<false>(XA, WA, X, W, PHI, OUT, WS, L, xcd, jx, tag);
  if (b == 255 && threadIdx.x < 64) {
    const GAS u64* f = (const GAS u64*)(WS + W_F) + threadIdx.x;
#pragma unroll 1
    for (int spin = 0; spin < SPIN_CAP; ++spin) {
      bool e = true;
#pragma unroll
      for (int i = 0; i < 4; ++i) e &= __hip_atomic_load(f + 64 * i, __ATOMIC_RELAXED, __HIP_MEMORY_SCOPE_AGENT) == tag;
      if (__all(e)) break;
      __builtin_amdgcn_s_sleep(2);
    }
    if (threadIdx.x == 0) __hip_atomic_store((GAS u64*)(WS + W_EPOCH), tag, __ATOMIC_RELAXED, __HIP_MEMORY_SCOPE_AGENT);
  }
}

extern "C" void kernel_launch(void* const* d_in, const int* in_sizes, int n_in, void* d_out, int out_size, void* d_ws,
                              size_t ws_size, hipStream_t stream) {
  Params P;
  P.x[0] = (const char*)d_in[0]; P.w[0] = (const char*)d_in[1]; P.wphi = (const char*)d_in[2]; P.wo[0] = (const char*)d_in[3];
  P.x[1] = (const char*)d_in[4]; P.w[1] = (const char*)d_in[5]; P.wo[1] = (const char*)d_in[6];
  P.out = (char*)d_out; P.ws = (char*)d_ws;
  (void)hipFuncSetAttribute((const void*)fused, hipFuncAttributeMaxDynamicSharedMemorySize, LDS_BYTES);
  hipLaunchKernelGGL(fused, dim3(256), dim3(768), LDS_BYTES, stream, P);
}
```

```cpp
#include <hip/hip_runtime.h>
#include <stdint.h>
#include <type_traits>
#pragma clang diagnostic ignored "-Winline-asm"

typedef short s16x4 __attribute__((ext_vector_type(4)));
typedef __bf16 bf16x8 __attribute__((ext_vector_type(8)));
typedef __bf16 bf16x2 __attribute__((ext_vector_type(2)));
typedef float f32x4 __attribute__((ext_vector_type(4)));
typedef float f32x2 __attribute__((ext_vector_type(2)));
typedef unsigned u32x4 __attribute__((ext_vector_type(4)));
typedef unsigned u32x2 __attribute__((ext_vector_type(2)));
typedef unsigned long long u64;
#define LAS __attribute__((address_space(3)))
#define GAS __attribute__((address_space(1)))
#define DEV __device__ __forceinline__
#define SB() __builtin_amdgcn_sched_barrier(0)

constexpr int SLOT = 50176;
constexpr int L_REC = 0;
constexpr int L_KM = L_REC, L_VM = L_REC + 17408;
constexpr int L_WOB = L_REC + 35328;
constexpr int L_WO = 57600;
constexpr int L_ZERO = 3 * SLOT;
constexpr int L_PT = L_ZERO + 64;
constexpr int L_KVT = L_PT + 1920;
constexpr int L_XM = L_PT;
constexpr int L_DUMMY = L_KVT + 2880;
constexpr int L_END = L_DUMMY + 256;
static_assert(L_END + 3072 <= 162816 && L_WO + 92160 <= L_ZERO && L_WO + 30720 <= 2 * SLOT && L_WOB + 61440 <= 2 * SLOT, "lds");
constexpr int LDS_BYTES = 163840;

constexpr int W_EPOCH = 0;
constexpr int W_F = 4096;
constexpr int W_KV = 65536;
constexpr int W_OB = 1048576;
constexpr int W_TOTAL = W_OB + 2 * 8 * 16 * 18432;
constexpr unsigned SENT = 0xFFFFFFFFu;
constexpr int OOB = 0x40000000;
constexpr int SPIN_CAP = 400000;
#define KVSLEEP 24
#define LDSLEEP 4
#define BLDSLEEP 30
#define BTH 0
#define BTN 4
#define NPRO 12
#define R2SLEEP 20
#define NRA 2
#define PFD 4
#define J3K 0
#define J3S 1
#define KW 3
#define LEAD0 3
#define LEADNA 2
#define LEADNB 2
#define LEADA 12
#define LEADB 12
#define HP 12
#define NWO1A 0
#define NWO1B 12
#define WSLA 0
#define WSLB 0
#define WS0A 0
#define WS0B 0
#define JB 0
#define CSLA 28
#define CSLB 28
#define ACN 0
#define ACK 12
#define CPF 3
#define OAUX 0
#define GD 4
#define HX 0
#define AJ 1
#define NWO2A 12
#define GSLEEP 2

template <int N> DEV void waitv() { asm volatile("s_waitcnt vmcnt(%0)" ::"n"(N) : "memory"); }
DEV void bar() { asm volatile("s_waitcnt lgkmcnt(0)\n\ts_barrier" ::: "memory"); }
template <int N> DEV void waitbar() { asm volatile("s_waitcnt vmcnt(%0)\n\ts_barrier" ::"n"(N) : "memory"); }

DEV unsigned pk2(float a, float b) {
  f32x2 v = {a, b};
  bf16x2 r = __builtin_convertvector(v, bf16x2);
  return __builtin_bit_cast(unsigned, r);
}
DEV bf16x8 asb(u32x4 v) { return __builtin_bit_cast(bf16x8, v); }
DEV f32x4 mfma(bf16x8 a, bf16x8 b, f32x4 c) { return __builtin_amdgcn_mfma_f32_16x16x32_bf16(a, b, c, 0, 0, 0); }
DEV f32x4 mf4(unsigned a0, unsigned a1, unsigned b0, unsigned b1, f32x4 c) {
  return __builtin_amdgcn_mfma_f32_4x4x4bf16_1k(__builtin_bit_cast(s16x4, u32x2{a0, a1}), __builtin_bit_cast(s16x4, u32x2{b0, b1}), c, 0, 0, 0);
}
DEV f32x4 mf16(unsigned a, unsigned b, f32x4 c) {
  return mfma(asb(u32x4{a, 0u, 0u, 0u}), asb(u32x4{b, 0u, 0u, 0u}), c);
}
DEV float dot2(unsigned a, unsigned b, float c) {
  return __builtin_amdgcn_fdot2_f32_bf16(__builtin_bit_cast(bf16x2, a), __builtin_bit_cast(bf16x2, b), c, false);
}
DEV u32x4 lds128(LAS char* L, int off) { return *(LAS u32x4*)(L + off); }
DEV u32x2 lds64(LAS char* L, int off) { return *(LAS u32x2*)(L + off); }
DEV unsigned lds32(LAS char* L, int off) { return *(LAS unsigned*)(L + off); }
DEV bf16x8 trfrag(LAS char* L, int off) {
  s16x4 a = __builtin_amdgcn_ds_read_tr16_b64_v4i16((LAS s16x4*)(L + off));
  s16x4 b = __builtin_amdgcn_ds_read_tr16_b64_v4i16((LAS s16x4*)(L + off + 1280));
  u32x2 ua = __builtin_bit_cast(u32x2, a), ub = __builtin_bit_cast(u32x2, b);
  u32x4 r = {ua.x, ua.y, ub.x, ub.y};
  return asb(r);
}
DEV void glds60(const char* base, unsigned voff, int dst) {
  asm volatile("s_mov_b32 m0, %2\n\ts_lshr_b64 exec, -1, 4\n\tglobal_load_lds_dwordx4 %0, %1\n\ts_mov_b64 exec, -1"
               ::"v"(voff), "s"(base), "s"(dst) : "memory", "m0", "scc");
}
DEV void glds64(const char* base, unsigned voff, int dst) {
  asm volatile("s_mov_b32 m0, %2\n\ts_nop 0\n\tglobal_load_lds_dwordx4 %0, %1" ::"v"(voff), "s"(base), "s"(dst) : "memory", "m0");
}
DEV void glds32(const char* base, unsigned voff, int dst) {
  asm volatile("s_mov_b32 m0, %2\n\ts_lshr_b64 exec, -1, 32\n\tglobal_load_lds_dwordx4 %0, %1\n\ts_mov_b64 exec, -1"
               ::"v"(voff), "s"(base), "s"(dst) : "memory", "m0", "scc");
}
template <int OFF> DEV void gload128(u32x4& d, const char* base, unsigned voff) {
  asm volatile("global_load_dwordx4 %0, %1, %2 offset:%3" : "=v"(d) : "v"(voff), "s"(base), "n"(OFF) : "memory");
}
DEV void gload64s(u32x2& d, const char* base, unsigned voff) {
  asm volatile("global_load_dwordx2 %0, %1, %2 sc1" : "=v"(d) : "v"(voff), "s"(base) : "memory");
}
#define STAMP(i) do {} while (0)
#define RSTAMP(i) do {} while (0)
DEV int opq(int v) { asm volatile("" : "+v"(v)); return v; }
DEV void swap16(unsigned& a, unsigned& b) {
  auto r = __builtin_amdgcn_permlane16_swap(a, b, false, false);
  a = r[0]; b = r[1];
}
DEV void swap32(unsigned& a, unsigned& b) {
  auto r = __builtin_amdgcn_permlane32_swap(a, b, false, false);
  a = r[0]; b = r[1];
}
DEV void swap16f(float& a, float& b) {
  unsigned x = __float_as_uint(a), y = __float_as_uint(b);
  swap16(x, y);
  a = __uint_as_float(x); b = __uint_as_float(y);
}
DEV unsigned canon(unsigned x) { return x < 0xFFFFFFFEu ? x : 0xFFFFFFFEu; }
DEV unsigned umax3(unsigned a, unsigned b, unsigned c) { unsigned m = a > b ? a : b; return m > c ? m : c; }
DEV u32x4 canon4(u32x4 v) { return u32x4{canon(v[0]), canon(v[1]), canon(v[2]), canon(v[3])}; }

struct Params {
  const char* x[2];
  const char* w[2];
  const char* wo[2];
  const char* wphi;
  char* out;
  char* ws;
};

DEV int kmap(int p) { return (p & ~12) | ((p & 4) << 1) | ((p & 8) >> 1); }
DEV int worow(int k) {
  int m = k + 4 * (((k >> 4) + 1) >> 1);
  int kr = k - 512;
  int r = 36 * (kr >> 2) + 16 + (kr & 3);
  return k < 512 ? m : r;
}

template <class G, class F>
DEV void features(LAS char* L, int pta, int ptb, bool act, const u32x4 (&m)[2], const unsigned (&e)[2], unsigned (&ph)[2][9], G&& pre, F&& done) {
  f32x4 P[2][3];
  u32x4 T0[3]; unsigned T1[3];
  const int a0 = act ? pta : L_ZERO, a1 = act ? ptb : L_ZERO, st = act ? 128 : 0;
#pragma unroll
  for (int t = 0; t < 3; ++t) { T0[t] = lds128(L, a0 + t * 4 * st); T1[t] = lds32(L, a1 + t * st); }
  pre();
  SB();
#pragma unroll
  for (int mt = 0; mt < 2; ++mt)
#pragma unroll
    for (int t = 0; t < 3; ++t) P[mt][t] = mfma(asb(T0[t]), asb(m[mt]), f32x4{0.f, 0.f, 0.f, 0.f});
#pragma unroll
  for (int mt = 0; mt < 2; ++mt)
#pragma unroll
    for (int t = 0; t < 3; ++t) P[mt][t] = mf16(T1[t], e[mt], P[mt][t]);
  const float LOG2E = 1.4426950408889634f;
  float p[2][9];
  f32x2 off[2];
#pragma unroll
  for (int mt = 0; mt < 2; ++mt) {
#pragma unroll
    for (int i = 0; i < 9; ++i) p[mt][i] = P[mt][i >> 2][i & 3];
    float a = p[mt][0], b = -p[mt][0];
#pragma unroll
    for (int i = 1; i < 9; ++i) { a = __builtin_fmaxf(a, p[mt][i]); b = __builtin_fmaxf(b, -p[mt][i]); }
    swap16f(a, b);
    float x = __builtin_amdgcn_fmed3f(a, b, 3.402823466e38f) * -LOG2E;
    float y = x;
    swap16f(x, y);
    off[mt] = f32x2{x, y};
  }
#pragma unroll
  for (int mt = 0; mt < 2; ++mt) {
    f32x2 ex[9], s;
#pragma unroll
    for (int i = 0; i < 9; ++i) {
      const f32x2 v = __builtin_elementwise_fma(f32x2{p[mt][i], p[mt][i]}, f32x2{LOG2E, -LOG2E}, off[mt]);
      ex[i] = f32x2{__builtin_amdgcn_exp2f(v[0]), __builtin_amdgcn_exp2f(v[1])};
      s = i ? s + ex[i] : ex[i];
    }
    float sp = s[0], sn = s[1];
    swap16f(sp, sn);
    float rp = __builtin_amdgcn_rcpf(sp + sn), rn = rp;
    swap16f(rp, rn);
#pragma unroll
    for (int i = 0; i < 9; ++i) {
      const f32x2 f = ex[i] * f32x2{rp, rn};
      ph[mt][i] = pk2(f[0], f[1]);
    }
    done(mt);
  }
}

template <int N, class F> DEV void sfor(F&& f) {
  if constexpr (N > 0) { sfor<N - 1>(f); f(std::integral_constant<int, N - 1>{}); }
}

DEV u64 next_tag(u64 t);
DEV u64 read_tag(const char* ws) {
  u64 t;
  asm volatile("s_load_dwordx2 %0, %1, 0x0\n\ts_waitcnt lgkmcnt(0)" : "=s"(t) : "s"(ws) : "memory");
  return next_tag(t);
}
DEV u64 next_tag(u64 t) { return (t ^ 0x9E3779B97F4A7C15ull) * 0xD1342543DE82EF95ull + 0x632BE59BD9B4E019ull; }

constexpr int J1 = 64 + 16 * NRA, NT = (414 - J1 + 15) / 16;
template <bool BOTH>
DEV void job(int n_, u64 pxa, u64 pwa, u64 pxb, u64 pwb, int xcd, int lane, u64& jb, unsigned& jo) {
  n_ = n_ < 413 ? n_ : 413;
  const int b2 = BOTH && n_ >= 207, r = n_ - 207 * b2, st = (r * 2850) >> 16, p = r - 23 * st;
  const int q = (p - 8) * 64 + lane, kr = (q * 4370) >> 16, r15 = q - 15 * kr, m = (r15 * 13) >> 6, kq = r15 - 5 * m;
  const u64 px = b2 ? pxb : pxa, pw = b2 ? pwb : pwa;
  jb = (p < 8 ? px : pw) + (u64)(unsigned)(p < 8 ? ((xcd >> 1) * 512 + p * 64) * 1152 + st * 128 : st * (64 * 3456) + (xcd & 1) * 512);
  jo = (unsigned)(p < 8 ? lane * 1152 : kr * 3456 + m * 1152 + kq * 128);
}
DEV void warm1(u64 jb, unsigned jo) {
  asm volatile("s_mov_b32 m0, %2\n\ts_nop 0\n\tglobal_load_lds_dword %0, %1" ::"v"(jo), "s"(jb), "s"(L_DUMMY) : "memory", "m0");
}

template <bool ISA>
DEV void loader(const char* XB, const char* W, const char* WO, const char* X1, const char* W1, char* WS, LAS char* L, int xcd, int jx, int wid) {
  const int rt = (xcd >> 1) * 2 + (jx >> 3), hp = (xcd & 1) * 8 + (jx & 7);
  const int lw = wid - 8;
  const char* X = XB + (size_t)(rt * 256 + lw * 64) * 1152;
  const int lane = threadIdx.x & 63;
  RSTAMP(30); STAMP(0);
  __builtin_amdgcn_s_setprio(2);
  __amdgpu_buffer_rsrc_t wsr = __builtin_amdgcn_make_buffer_rsrc(WS, 0, W_TOTAL, 0x00020000);
  u64 tag = 0;
  if (ISA) { if (LDSLEEP) __builtin_amdgcn_s_sleep(LDSLEEP); }
  else {
    const u32x4 s4 = {SENT, SENT, SENT, SENT};
    const int ob = W_OB + rt * 16 * 18432, u = lw * 64 + lane;
#pragma unroll
    for (int i = 0; i < 8; ++i)
      __builtin_amdgcn_raw_buffer_store_b128(s4, wsr, ob + lw * 4 * 18432 + hp * 1024 + lane * 16, (i & 3) * 18432 + (i >> 2) * (8 * 16 * 18432), 16);
    __builtin_amdgcn_raw_buffer_store_b128(s4, wsr, ob + (u >> 7) * (8 * 16 * 18432) + ((u >> 3) & 15) * 18432 + 16384 + hp * 128 + (u & 7) * 16, 0, 16);
    __builtin_amdgcn_raw_buffer_store_b128(s4, wsr, u < 180 ? W_KV + (hp * 8 + rt) * 2880 + u * 16 : OOB, 0, 16);
    STAMP(21);
    tag = read_tag(WS);
    if (BLDSLEEP) __builtin_amdgcn_s_sleep(BLDSLEEP);
  }
  const int r8 = lane >> 3;
  const unsigned xo0 = (unsigned)(2 * r8 * 1152 + (((lane & 7) ^ (r8 >> 1)) * 16));
  const unsigned xo1 = (xo0 ^ 64u) + 16 * 1152;
  const int dW = lw * 3840, dX = 15360 + lw * 8192;
  auto opX = [&](int s, int i) {
    glds64(X + s * 128 + ((i >> 1) & 1) * 1152 + (i >> 2) * (32 * 1152), (i & 1) ? xo1 : xo0, (s % 3) * SLOT + dX + i * 1024);
  };
#pragma unroll
  for (int i = 0; i < 4; ++i) opX(0, i);
  SB();
  const int r6 = (lane * 26) >> 8, un = lane - 10 * r6;
  const int part = un >= 5, u5 = un - 5 * part;
  const int wcol = ((hp * 72) & ~15) + u5 * 16;
  int kap[4];
  unsigned wj[4];
#pragma unroll
  for (int j = 0; j < 4; ++j) {
    kap[j] = kmap(24 * lw + 6 * j + r6);
    wj[j] = (unsigned)(kap[j] < 64 ? wcol + (1 + part) * 1152 + kap[j] * 3456 : wcol + (kap[j] - 64 + 32 * part) * 3456);
  }
  auto opW = [&](int s, int j) { glds60(W + s * (64 * 3456), wj[j], (s % 3) * SLOT + dW + j * 960); };
#pragma unroll
  for (int j = 0; j < 4; ++j) opW(0, j);
#pragma unroll
  for (int i = 4; i < 8; ++i) opX(0, i);
  const int g = lane >> 4, c = lane & 15;
  const char* XH = XB + (size_t)(rt * 256 - (rt > 0 ? 16 : 0)) * 1152;
  unsigned ho = 0;
  if (!ISA) {
    const int hc = 4 * lw + (r8 & 3);
    ho = (unsigned)((rt > 0 ? (2 * (hc & 7) + (hc >> 3)) * 1152 : 0) + (((lane & 7) ^ (hc >> 1)) * 16));
  }
  auto opH = [&](int s) { glds32(XH + s * 128, ho, (s % 3) * SLOT + 48128 + lw * 512); };
  if (!ISA) opH(0);
  const int tq = (lane >> 2) & 3, tp = lane & 3;
  const int wrow = (16 * (g >> 1) + 4 * (g & 1) + tq) * 160 + (hp & 1) * 8;
  const int hwa = wrow + 16 * (tp & 1) + 40 * (tp >> 1) + (lw >> 1) * 80 + (lw & 1) * 8;
  const int hwx = wrow + 32 + (tp == 1 ? 80 : 0);
  const int hxa = 48128 + c * 128 + ((g ^ (c >> 1)) * 16);
  f32x4 hacc = {0.f, 0.f, 0.f, 0.f}, hacx = hacc;
  bf16x8 hw[2], hw6[2];
  u32x4 hxf[2];
  auto hread = [&](int s) {
    if ((HX & 1) && s < 8) return;
    const int o = (s % 3) * SLOT;
    hxf[0] = lds128(L, o + hxa); hxf[1] = lds128(L, (o + hxa) ^ 64);
    hw[0] = trfrag(L, o + hwa); hw[1] = trfrag(L, o + hwa + 5120);
    if (lw == 0) { hw6[0] = trfrag(L, o + hwx); hw6[1] = trfrag(L, o + hwx + 5120); }
  };
  auto hmul = [&]() {
    hacc = mfma(hw[0], asb(hxf[0]), hacc); hacc = mfma(hw[1], asb(hxf[1]), hacc);
    if (lw == 0) { hacx = mfma(hw6[0], asb(hxf[0]), hacx); hacx = mfma(hw6[1], asb(hxf[1]), hacx); }
  };
  const int rto = xcd, cg = jx & 7;
  const int wocol = cg * 144 + (un > 8 ? 8 : un) * 16;
  unsigned wo[4], wl[4];
  auto opO = [&](int n) {
    const int bk = n >> 2, j = n & 3;
    glds60(WO + (bk < 5 ? 108 * bk * 1152 : 0), bk < 5 ? wo[j] : wl[j], (ISA ? L_WO : L_WOB) + bk * 15360 + dW + j * 960);
  };
  unsigned fo = 0, ajo[2];
  u64 ajb[2];
  u32x2 fv = {0u, 0u};
  sfor<8>([&](auto S) {
    constexpr int s = S + 1;
    if constexpr (s == 3) {
#pragma unroll
      for (int j = 0; j < 4; ++j) { wo[j] = (unsigned)opq(worow(kap[j]) * 1152 + wocol); wl[j] = (unsigned)opq(worow(480 + kap[j]) * 1152 + wocol); }
      if constexpr (ISA && AJ) {
#pragma unroll
        for (int i = ACN / 4; i < 2; ++i) {
          job<true>(J1 + ((NT - 8 > NPRO ? NT - 8 : NPRO) + i * 4 + lw) * 16 + jx, 0, 0, (u64)X1, (u64)W1, xcd, lane, ajb[i], ajo[i]);
          asm volatile("" : "+v"(ajo[i]), "+s"(ajb[i]));
        }
      }
      fo = (unsigned)opq(W_F + 1024 + (lane < 16 ? rto * 16 + lane : (lane & 7) * 16 + hp) * 8);
    }
    sfor<(ISA ? 12 : 13)>([&](auto I) {
      constexpr int i = I - (!ISA && I > HP);
      if constexpr (!ISA && I == HP) opH(s);
      else if constexpr (i % 3 < 2) opX(s, 2 * (i / 3) + i % 3); else opW(s, i / 3);
      constexpr int ld = ISA ? (s <= LEADNA ? LEAD0 : LEADA) : (s <= LEADNB ? LEAD0 : LEADB);
      if constexpr (I == ld - 1) {
        if constexpr (s < 6) { STAMP(s == 1 ? 1 : s == 2 ? 4 : s == 3 ? 7 : s == 4 ? 9 : 10); }
        if constexpr (!ISA && s > 1 && !(HX & 1)) { SB(); hmul(); SB(); }
        waitbar<ld>();
        if constexpr (!ISA) { hread(s - 1); SB(); }
        if constexpr (s < 5) { STAMP(s == 1 ? 2 : 9 + s); }
      }
    });
  });
  if constexpr (!ISA && !(HX & 1)) { SB(); hmul(); SB(); }
  waitbar<0>();
  if constexpr (!ISA) { hread(8); SB(); }
  gload64s(fv, WS, fo);
  static_assert(NWO1A <= 8 && NWO1B <= 16, "");
  constexpr int NWM = 2 + (ISA && AJ ? 2 - ACN / 4 : 0), NW1 = ISA ? NWO1A : NWO1B, NW2 = ISA ? NWO2A : 24;
  {
    if ((ISA ? WS0A : WS0B) > 0) __builtin_amdgcn_s_sleep(ISA ? WS0A : WS0B);
    if constexpr (ISA && AJ && ACN < 4) warm1(ajb[0], ajo[0]);
    if constexpr (ISA && AJ && ACN < 8) warm1(ajb[1], ajo[1]);
    const u64 jb = (u64)(WO + (jx * 4 + lw) * (81 * 128));
    warm1(jb, lane * 128);
    warm1(jb, (lane < 16 ? lane + 64 : 80) * 128);
    if ((ISA ? WSLA : WSLB) > 0) __builtin_amdgcn_s_sleep(ISA ? WSLA : WSLB);
  }
  STAMP(5);
  if constexpr (!ISA) {
    SB();
    hmul();
    const int G = (g & 2) + (c >> 3), hr = c & 7;
    const int hb = lw < 2 ? L_KM + G * 4352 + (g & 1) * 2176 + hr * 16 + lw * 8 : L_VM + G * 4480 + (2 * (g & 1) + (lw & 1)) * 1088 + hr * 8;
    *(LAS u32x2*)(L + hb) = u32x2{pk2(hacc[0], hacc[1]), pk2(hacc[2], hacc[3])};
    if (lw == 0 && g < 2) {
      const int xb = L_XM + (c >> 3) * 1152 + hr * 8 + g * 4;
      *(LAS unsigned*)(L + xb) = pk2(hacx[0], hacx[1]);
      *(LAS unsigned*)(L + xb + 2304) = pk2(hacx[2], hacx[3]);
    }
  }
  SB();
#pragma unroll
  for (int n = 0; n < NW1; ++n) opO(n);
  if (ISA && lw == 3) tag = read_tag(WS);
  asm volatile("s_waitcnt vmcnt(%1)" : "+v"(fv) : "n"(NWM + NW1) : "memory");
  if (ISA && lw == 3) {
    __builtin_amdgcn_raw_buffer_store_b64(u32x2{(unsigned)tag, (unsigned)(tag >> 32)}, wsr, lane == 0 ? W_F + (rt * 16 + hp) * 8 : OOB, 0, 16);
  }
  if (lw == 3 && !__all(fv[0] == (unsigned)tag && fv[1] == (unsigned)(tag >> 32))) {
    const GAS u64* fp = (const GAS u64*)(WS + fo);
#pragma unroll 1
    for (int spin = 0; spin < SPIN_CAP; ++spin) {
      u64 v = __hip_atomic_load(fp, __ATOMIC_RELAXED, __HIP_MEMORY_SCOPE_AGENT);
      if (__all(v == tag)) break;
      __builtin_amdgcn_s_sleep(2);
    }
  }
  STAMP(6);
  bar();
  STAMP(8);
#pragma unroll
  for (int n = NW1; n < NW2; ++n) opO(n);
  if (ISA && lw < 3) {
    const int u = lw * 64 + lane;
    const int kvl = u < 180 ? W_KV + hp * 8 * 2880 + u * 16 : OOB;
    u32x4 pt[8];
    unsigned pend = 0xFFu;
    __builtin_amdgcn_s_sleep(KVSLEEP);
#pragma unroll 1
    for (int spin = 0; spin < SPIN_CAP; ++spin) {
#pragma unroll
      for (int i = 0; i < 8; ++i)
        if (pend >> i & 1) pt[i] = __builtin_amdgcn_raw_buffer_load_b128(wsr, kvl + i * 2880, 0, 16);
      unsigned np = 0;
#pragma unroll
      for (int i = 0; i < 8; ++i) {
        unsigned m = umax3(pt[i][0], pt[i][1], pt[i][2]);
        m = m > pt[i][3] ? m : pt[i][3];
        np |= __any(m == SENT) ? 1u << i : 0u;
      }
      pend = np;
      if (!pend) break;
      __builtin_amdgcn_s_sleep(1);
    }
    STAMP(14);
    float s[8];
    const unsigned selLo = (unsigned)opq(0x00003F80), selHi = (unsigned)opq(0x3F800000);
#pragma unroll
    for (int j = 0; j < 8; ++j) s[j] = 0.f;
#pragma unroll
    for (int i = 0; i < 8; ++i) {
#pragma unroll
      for (int j = 0; j < 4; ++j) {
        s[2 * j] = dot2(pt[i][j], selLo, s[2 * j]);
        s[2 * j + 1] = dot2(pt[i][j], selHi, s[2 * j + 1]);
      }
    }
    if (u < 180) *(LAS u32x4*)(L + L_KVT + u * 16) = u32x4{pk2(s[0], s[1]), pk2(s[2], s[3]), pk2(s[4], s[5]), pk2(s[6], s[7])};
  }
  if constexpr (ISA) {
    bar();
#pragma unroll
    for (int n = NW2; n < 24; ++n) opO(n);
  }
  waitv<0>();
  STAMP(15);
  bar();
}

template <bool ISA>
DEV void body(const char* XA, const char* WA, const char* XB, const char* W, const char* PHI, char* OUT, char* WS, LAS char* L, int xcd, int jx, u64& tag) {
  const int br = ISA ? 0 : 1;
  const int rt = (xcd >> 1) * 2 + (jx >> 3), hp = (xcd & 1) * 8 + (jx & 7);
  const int tid = threadIdx.x;
  const int wid = __builtin_amdgcn_readfirstlane(tid >> 6);
  int lane = tid & 63;
  int g = lane >> 4, c = lane & 15;
  RSTAMP(30); STAMP(0);

  u64 jb; unsigned jo;
  const u64 pxa = (u64)XA, pwa = (u64)WA, pxb = (u64)XB, pwb = (u64)W;
#define JOB(N) job<!ISA>(N, pxa, pwa, pxb, pwb, xcd, lane, jb, jo)
  auto warm = [&]() {
    asm volatile("s_mov_b32 m0, %2\n\ts_nop 0\n\tglobal_load_lds_dword %0, %1" ::"v"(jo), "s"(jb), "s"(L_DUMMY) : "memory", "m0");
  };
  auto warmw = [&](int w) {
    asm volatile("s_cmp_eq_u32 %3, %4\n\ts_cselect_b64 exec, -1, 0\n\ts_setprio 3\n\ts_mov_b32 m0, %2\n\ts_nop 0\n\tglobal_load_lds_dword %0, %1\n\ts_setprio 0\n\ts_mov_b64 exec, -1"
                 ::"v"(jo), "s"(jb), "s"(L_DUMMY), "s"(wid), "n"(w) : "memory", "m0", "scc");
  };
  static_assert((AJ || NT - NPRO <= 8) && NPRO <= NT && J1 + NPRO * 16 >= 207, "");
  if (wid < (ISA ? NRA : 4)) {
    JOB(wid < NRA ? wid * 32 + br * 16 + jx : NRA * 32 + (wid - NRA) * 16 + jx);
    if (wid > 0) __builtin_amdgcn_s_sleep(PFD);
    if (wid > 1) __builtin_amdgcn_s_sleep(PFD);
    if (wid > 2) __builtin_amdgcn_s_sleep(PFD);
    warm();
  }
  if (!ISA) {
    const int t1 = (wid + 4) & 7;
    __builtin_amdgcn_s_sleep(R2SLEEP);
    tag = read_tag(WS);
#pragma unroll
    for (int i = 0; i < 3; ++i)
      if (t1 + 8 * i < NPRO) { JOB(J1 + (t1 + 8 * i) * 16 + jx); warm(); }
    if (wid == 3 && jx < 3 && xcd >= 2) {
      const int st = jx * 4 + g;
      jb = pxb + (u64)(unsigned)(((xcd >> 1) * 512 - 16) * 1152);
      jo = (unsigned)(c * 1152 + (st < 8 ? st : 8) * 128);
      warm();
    }
    if (!AJ) {
      JOB(J1 + (NPRO + ((t1 - NPRO) & 7)) * 16 + jx);
      asm volatile("" : "+v"(jo), "+s"(jb));
    }
  }
  if (ISA && ACN > 0) {
    job<true>(J1 + (NPRO + wid) * 16 + jx, pxa, pwa, pxb, pwb, xcd, lane, jb, jo);
    asm volatile("" : "+v"(jo), "+s"(jb));
  }
  const int dX = 15360 + wid * 4096;
  __amdgpu_buffer_rsrc_t wsr = __builtin_amdgcn_make_buffer_rsrc(WS, 0, W_TOTAL, 0x00020000);
  unsigned phv = 0, phw = 0;
  __amdgpu_buffer_rsrc_t pr = __builtin_amdgcn_make_buffer_rsrc((void*)(PHI + hp * 1296), 0, 1296, 0x00020000);
  int phs = 0;
  if (ISA) {
    int st = tid >= 384, e3 = tid - 384 * st;
    int t = st ? e3 >> 5 : e3 >> 7;
    int a = st ? e3 & 31 : (e3 >> 2) & 31;
    int gg = a >> 3, f = 9 * ((a >> 2) & 1) + 4 * t + (a & 3);
    int d = st ? (gg == 0 ? 16 : 0) : (8 * (gg & 1) + 2 * (gg >> 1) + 2 * (e3 & 3));
    bool ok = (4 * t + (a & 3) < 9) && !(st && (gg & 1)) && tid < 480;
    phs = opq(ok ? (((gg >> 1) * 18 + d) * 18 + f) * 2 : OOB);
  }
  auto phload = [&]() {
    asm volatile("s_setprio 3\n\tbuffer_load_ushort %0, %2, %3, 0 offen\n\tbuffer_load_ushort %1, %2, %3, 0 offen offset:36\n\ts_setprio 0"
                 : "=&v"(phv), "=&v"(phw) : "v"(phs), "s"(pr) : "memory");
  };
  const int obt = W_OB + (br * 8 + rt) * 16 * 18432;
  int obm = obt + 2 * wid * 18432 + hp * 1024 + lane * 16;
  const int xb = W_KV + (hp * 8 + rt) * 2880;
  const int flo = tid == 448 ? W_F + ((br * 8 + rt) * 16 + hp) * 8 : OOB;
  if (ISA) phload();
  if (tid < 16) *(LAS unsigned*)(L + L_ZERO + tid * 4) = (tid & 13) == 12 ? 0x00003F80u : 0u;

  int tq = (lane >> 2) & 3, tp = lane & 3;
  int wrow = (16 * (g >> 1) + 4 * (g & 1) + tq) * 160;
  int unit = 16 * (tp & 1) + 40 * (tp >> 1);
  const int wa = wrow + unit + (hp & 1) * 8;
  const int wx0 = wrow + 32 + (hp & 1) * 8 + (tp == 1 ? 80 : tp == 2 ? 10240 : 0);
  const int wx1 = wx0 + (tp == 2 ? 80 : 5120);
  const int rto = xcd, hf = jx >> 3, cg = jx & 7;

  f32x4 acc[7][2];
#pragma unroll
  for (int t = 0; t < 7; ++t) { acc[t][0] = f32x4{0.f, 0.f, 0.f, 0.f}; acc[t][1] = acc[t][0]; }

  STAMP(1);
  bar();
  STAMP(2); RSTAMP(25);
  bf16x8 wf[2][7];
  u32x4 xf[2][2];
  int wa_s[3], wx_s[3][2], xa_s[3];
#pragma unroll
  for (int i = 0; i < 3; ++i) {
    wa_s[i] = opq(wa + i * SLOT); wx_s[i][0] = opq(wx0 + i * SLOT); wx_s[i][1] = opq(wx1 + i * SLOT);
    xa_s[i] = opq(i * SLOT + dX + c * 128 + ((g ^ (c >> 1)) * 16));
  }
  auto rdW = [&](auto KN, auto T) {
    constexpr int kn = KN, t = T, sl = (kn >> 1) % 3, od = kn & 1;
    if constexpr (t < 4) return trfrag(L, wa_s[sl] + od * 5120 + (t >> 1) * 80 + (t & 1) * 8);
    else if constexpr (t < 6) return trfrag(L, wa_s[sl] + 10240 + od * 80 + (t & 1) * 8);
    else return trfrag(L, wx_s[sl][od]);
  };
  auto rdX = [&](auto KN, int mt) {
    constexpr int kn = KN, sl = (kn >> 1) % 3, od = kn & 1;
    return lds128(L, (od ? xa_s[sl] ^ 64 : xa_s[sl]) + mt * 2048);
  };
  using I0 = std::integral_constant<int, 0>;
  xf[0][0] = rdX(I0{}, 0); xf[0][1] = rdX(I0{}, 1);
  sfor<7>([&](auto T) { wf[0][T] = rdW(I0{}, T); });
  sfor<18>([&](auto K) {
    constexpr int k = K, cur = k & 1, nxt = cur ^ 1, kn = k + 1;
    using KN = std::integral_constant<int, kn>;
    if constexpr (ISA && k == KW) waitv<0>();
    if constexpr ((k & 1) && kn < 18) bar();
    if constexpr (ISA && k == KW) {
      asm volatile("" : "+v"(phv), "+v"(phw));
      if (tid < 480) *(LAS unsigned*)(L + L_PT + tid * 4) = phv | (phw << 16);
    }
    sfor<7>([&](auto T) {
      constexpr int t = T;
      SB();
      acc[t][0] = mfma(wf[cur][t], asb(xf[cur][0]), acc[t][0]);
      if constexpr (kn < 18) {
        if constexpr (t == 0) { xf[nxt][0] = rdX(KN{}, 0); xf[nxt][1] = rdX(KN{}, 1); }
        wf[nxt][t] = rdW(KN{}, T);
      }
      SB();
      acc[t][1] = mfma(wf[cur][t], asb(xf[cur][1]), acc[t][1]);
      if constexpr (!ISA && k == 0 && t == 0) __builtin_amdgcn_raw_buffer_store_b64(u32x2{(unsigned)tag, (unsigned)(tag >> 32)}, wsr, flo, 0, 16);
      if constexpr (ISA && ACN > 0 && t == 1 && k == ACK)
        asm volatile("s_cmp_lt_u32 %3, %4\n\ts_cselect_b64 exec, -1, 0\n\ts_mov_b32 m0, %2\n\ts_nop 0\n\tglobal_load_lds_dword %0, %1\n\ts_mov_b64 exec, -1"
                     ::"v"(jo), "s"(jb), "s"(L_DUMMY), "s"(wid), "n"(ACN) : "memory", "m0", "scc");
      if constexpr (!ISA && !AJ && JB && t == 1 && k == J3K && NPRO < NT)
        asm volatile("s_cmp_lt_u32 %3, %4\n\ts_cselect_b64 exec, -1, 0\n\ts_mov_b32 m0, %2\n\ts_nop 0\n\tglobal_load_lds_dword %0, %1\n\ts_mov_b64 exec, -1"
                     ::"v"(jo), "s"(jb), "s"(L_DUMMY), "s"((wid + 4 - NPRO) & 7), "n"(NT - NPRO) : "memory", "m0", "scc");
      if constexpr (!ISA && !AJ && !JB && t == 1 && k >= J3K && (k - J3K) % J3S == 0 && NPRO + (k - J3K) / J3S < NT) warmw((4 + NPRO + (k - J3K) / J3S) & 7);
    });
    SB();
    if constexpr (k == 5 || k == 11) STAMP(3 + (k == 11));
  });
  STAMP(5); RSTAMP(26);
  lane = opq(tid) & 63; g = lane >> 4; c = lane & 15; tq = (lane >> 2) & 3; tp = lane & 3;
  const bool act = (c >> 3) == (g >> 1);
  const int pta = L_PT + (g * 8 + (c & 7)) * 16;
  const int ptb = L_PT + 1536 + (g * 8 + (c & 7)) * 4;

  u32x4 km[2], qm[2];
  unsigned ex[2], qx[2];
#pragma unroll
  for (int mt = 0; mt < 2; ++mt) {
    km[mt] = u32x4{pk2(acc[0][mt][0], acc[0][mt][1]), pk2(acc[0][mt][2], acc[0][mt][3]), pk2(acc[1][mt][0], acc[1][mt][1]), pk2(acc[1][mt][2], acc[1][mt][3])};
    qm[mt] = u32x4{pk2(acc[4][mt][0], acc[4][mt][1]), pk2(acc[4][mt][2], acc[4][mt][3]), pk2(acc[5][mt][0], acc[5][mt][1]), pk2(acc[5][mt][2], acc[5][mt][3])};
    ex[mt] = pk2(acc[6][mt][0], acc[6][mt][1]);
    qx[mt] = pk2(acc[6][mt][2], acc[6][mt][3]);
    swap32(ex[mt], qx[mt]);
    if (ISA) qx[mt] = (g & 1) ? 0u : qx[mt];
  }
  u32x4 om[2]; unsigned ox[2];
  if (ISA) {
    STAMP(6);
    const int rbase = L_REC + g * 12288;
    const int pc = g & 1;
    {
      unsigned ph[2][9];
      unsigned ek[2] = {pc ? 0u : ex[0], pc ? 0u : ex[1]};
      features(L, pta, ptb, act, km, ek, ph, [&]() {
#pragma unroll
    for (int mt = 0; mt < 2; ++mt) {
      const int rp = 32 * wid + 16 * mt + c;
      *(LAS u32x4*)(L + rbase + 8192 + (rp ^ (8 * pc)) * 16) =
          u32x4{pk2(acc[2][mt][0], acc[2][mt][1]), pk2(acc[2][mt][2], acc[2][mt][3]), pk2(acc[3][mt][0], acc[3][mt][1]), pk2(acc[3][mt][2], acc[3][mt][3])};
    }
      }, [&](int mt) {
        const int rp = 32 * wid + 16 * mt + c;
        *(LAS u32x4*)(L + rbase + rp * 16) = u32x4{ph[mt][0], ph[mt][1], ph[mt][2], ph[mt][3]};
        *(LAS u32x4*)(L + rbase + 4096 + (rp ^ 8) * 16) = u32x4{ph[mt][4], ph[mt][5], ph[mt][6], ph[mt][7]};
        *(LAS u32x2*)(L + L_REC + 49152 + g * 2112 + rp * 8) = u32x2{ph[mt][8], ex[mt]};
      });
    }
    STAMP(7);
    bar();
    STAMP(8);
    {
      const int hd = wid & 1, nt = (wid >> 1) & 1;
      const int rowb = 16 * (g >> 1) + 4 * (g & 1) + tq;
      const int hb = L_REC + hd * 2 * 12288;
      const int sel = tp >> 1;
      const int aM0 = hb + (tp & 1) * 8 + sel * 4096 + (rowb + 8 * sel) * 16;
      const int aN0 = hb + (tp & 1) * 8 + 8192 + sel * 12288 + (rowb + 8 * sel) * 16;
      const int aS = L_REC + 49152 + (2 * hd + (tp & 1)) * 2112 + rowb * 8;
      const int a = (c >> 3) * 8 + (c & 7);
      const int b = (c >= 2 && c < 6) ? 14 + c : -1;
      const int dI = nt == 0 ? a : b;
      const int n0 = nt == 0 ? aN0 : (tp == 0 ? aS + 2112 : L_ZERO + 48);
      const int n1 = nt == 0 ? aN0 ^ 128 : (tp == 0 ? n0 + 64 : n0);
      const int nst = nt == 0 ? 512 : (tp == 0 ? 256 : 0);
      auto tr2 = [&](int p, int q) {
        s16x4 u = __builtin_amdgcn_ds_read_tr16_b64_v4i16((LAS s16x4*)(L + p));
        s16x4 v = __builtin_amdgcn_ds_read_tr16_b64_v4i16((LAS s16x4*)(L + q));
        u32x2 ua = __builtin_bit_cast(u32x2, u), ub = __builtin_bit_cast(u32x2, v);
        return asb(u32x4{ua.x, ua.y, ub.x, ub.y});
      };
      f32x4 D0 = {0.f, 0.f, 0.f, 0.f}, D1 = D0;
      bf16x8 nf[8];
#pragma unroll
      for (int ks = 0; ks < 8; ++ks) nf[ks] = tr2(n0 + ks * nst, n1 + ks * nst);
      if (wid < 4) {
        bf16x8 m0[8], m1[8];
#pragma unroll
        for (int ks = 0; ks < 8; ++ks) {
          m0[ks] = tr2(aM0 + ks * 512, (aM0 ^ 128) + ks * 512);
          m1[ks] = tr2(aM0 + 12288 + ks * 512, (aM0 ^ 128) + 12288 + ks * 512);
        }
#pragma unroll
        for (int ks = 0; ks < 8; ++ks) { D0 = mfma(m0[ks], nf[ks], D0); D1 = mfma(m1[ks], nf[ks], D1); }
        unsigned a0 = pk2(D0[0], D0[1]), a1 = pk2(D0[2], D0[3]), b0 = pk2(D1[0], D1[1]), b1 = pk2(D1[2], D1[3]);
        swap16(a0, b0); swap16(a1, b1);
        const int kvm = dI >= 0 ? xb + hd * 1440 + dI * 64 + (((g & 1) << 1) | (g >> 1)) * 16 : OOB;
        __builtin_amdgcn_raw_buffer_store_b128(canon4(u32x4{a0, a1, b0, b1}), wsr, kvm, 0, 16);
      } else {
        bf16x8 m0[8];
#pragma unroll
        for (int ks = 0; ks < 8; ++ks) m0[ks] = tr2(aS + ks * 256, aS + 64 + ks * 256);
#pragma unroll
        for (int ks = 0; ks < 8; ks += 2) { D0 = mfma(m0[ks], nf[ks], D0); D1 = mfma(m0[ks + 1], nf[ks + 1], D1); }
        unsigned t0 = pk2(D0[0] + D1[0], D0[1] + D1[1]), t1 = t0;
        swap16(t0, t1);
        const int kvx = (dI >= 0 && g == 0) ? xb + hd * 1440 + 1280 + dI * 8 : OOB;
        __builtin_amdgcn_raw_buffer_store_b64(u32x2{canon(t0), canon(t1)}, wsr, kvx, 0, 16);
      }
    }
    STAMP(9);
    unsigned ph[2][9];
    features(L, pta, ptb, act, qm, qx, ph, [&]() {}, [&](int) {});
    STAMP(13);
    bar();
    STAMP(15);
    const int hb = L_KVT + (g >> 1) * 1440;
    f32x4 O[2][3];
#pragma unroll
    for (int t = 0; t < 3; ++t) {
      const int d = t < 2 ? ((c >> 2) & 1) * 8 + 4 * t + (c & 3) : 16 + (c & 3);
      const int a0 = act ? hb + d * 64 + (g & 1) * 32 : L_ZERO;
      const int a2 = act ? hb + 1280 + d * 8 + (g & 1) * 4 : L_ZERO;
      u32x4 T0 = lds128(L, a0), T1 = lds128(L, a0 + 16);
      const unsigned T2 = lds32(L, a2);
#pragma unroll
      for (int mt = 0; mt < 2; ++mt) {
        f32x4 z = {0.f, 0.f, 0.f, 0.f};
        z = mfma(asb(T0), asb(u32x4{ph[mt][0], ph[mt][1], ph[mt][2], ph[mt][3]}), z);
        z = mfma(asb(T1), asb(u32x4{ph[mt][4], ph[mt][5], ph[mt][6], ph[mt][7]}), z);
        O[mt][t] = mf16(T2, ph[mt][8], z);
      }
    }
#pragma unroll
    for (int mt = 0; mt < 2; ++mt) {
      float rd = __builtin_amdgcn_rcpf(O[mt][2][2] + 1e-6f);
      om[mt] = u32x4{pk2(O[mt][0][0] * rd, O[mt][0][1] * rd), pk2(O[mt][0][2] * rd, O[mt][0][3] * rd), pk2(O[mt][1][0] * rd, O[mt][1][1] * rd), pk2(O[mt][1][2] * rd, O[mt][1][3] * rd)};
      ox[mt] = pk2(O[mt][2][0] * rd, O[mt][2][1] * rd);
    }
  } else {
    const int rec = 8 + 16 * wid + c;
    STAMP(6);
    const int vb = L_VM + (g >> 1) * 8960 + (g & 1) * 2176 + rec * 8;
#pragma unroll
    for (int mt = 0; mt < 2; ++mt) {
      *(LAS u32x2*)(L + vb + mt * 4480) = u32x2{pk2(acc[2][mt][0], acc[2][mt][1]), pk2(acc[2][mt][2], acc[2][mt][3])};
      *(LAS u32x2*)(L + vb + mt * 4480 + 1088) = u32x2{pk2(acc[3][mt][0], acc[3][mt][1]), pk2(acc[3][mt][2], acc[3][mt][3])};
    }
    auto swap4 = [&](u32x4& a, u32x4& b) {
#pragma unroll
      for (int i = 0; i < 4; ++i) { unsigned x = a[i], y = b[i]; swap16(x, y); a[i] = x; b[i] = y; }
    };
    swap4(km[0], km[1]);
    swap16(ex[0], ex[1]);
    const int ka = L_KM + g * 4352 + rec * 16, xa = L_XM + g * 1152 + rec * 8;
    *(LAS u32x4*)(L + ka) = km[0];
    *(LAS u32x4*)(L + ka + 2176) = km[1];
    *(LAS u32x2*)(L + xa) = u32x2{ex[0], ex[1]};
    swap4(qm[0], qm[1]);
    swap16(qx[0], qx[1]);
    const f32x4 z4 = {0.f, 0.f, 0.f, 0.f};
    auto qk = [&](u32x4 k0, u32x4 k1, u32x2 kx) {
      f32x4 s = mf4(k0[0], k0[1], qm[0][0], qm[0][1], z4);
      s = mf4(k0[2], k0[3], qm[0][2], qm[0][3], s);
      s = mf4(k1[0], k1[1], qm[1][0], qm[1][1], s);
      s = mf4(k1[2], k1[3], qm[1][2], qm[1][3], s);
      return mf4(kx[0], kx[1], qx[0], 0u, s);
    };
    f32x4 S2 = qk(km[0], km[1], u32x2{ex[0], ex[1]});
    const int j = c & 3;
    const unsigned m0 = j < 1 ? 0xFFFF0000u : 0u, m1 = j < 2 ? 0xFFFFFFFFu : (j < 3 ? 0xFFFF0000u : 0u);
    const int ti = (16 * wid + 4 * tp + tq) * 8;
    const int va = opq(L_VM + g * 4480 + ti), vx = opq(L_XM + g * 1152 + ti);
    STAMP(7);
    bar();
    STAMP(8);
    STAMP(13);
    u32x4 K0[2], K1[2]; u32x2 KX[2], VT[5][3];
#pragma unroll
    for (int kb = 0; kb < 2; ++kb) {
      K0[kb] = lds128(L, ka - 128 + kb * 64); K1[kb] = lds128(L, ka + 2176 - 128 + kb * 64);
      KX[kb] = lds64(L, xa - 64 + kb * 32);
    }
#pragma unroll
    for (int kb = 0; kb < 3; ++kb)
#pragma unroll
      for (int d = 0; d < 5; ++d)
        VT[d][kb] = __builtin_bit_cast(u32x2, __builtin_amdgcn_ds_read_tr16_b64_v4i16((LAS s16x4*)(L + (d < 4 ? va + d * 1088 : vx) + kb * 32)));
    f32x4 S0 = qk(K0[0], K1[0], KX[0]), S1 = qk(K0[1], K1[1], KX[1]);
    if (rt == 0 && wid == 0) {
#pragma unroll
      for (int i = 0; i < 4; ++i) { S0[i] = c < 8 ? -4.242640687e9f : S0[i]; S1[i] = c < 4 ? -4.242640687e9f : S1[i]; }
    }
    float lg[8] = {S2[0], j < 1 ? S0[1] : S2[1], j < 2 ? S0[2] : S2[2], j < 3 ? S0[3] : S2[3], S1[0], S1[1], S1[2], S1[3]};
    float mx = lg[0];
#pragma unroll
    for (int sl = 1; sl < 8; ++sl) mx = __builtin_fmaxf(mx, lg[sl]);
    const float SC = 0.23570226039551584f * 1.4426950408889634f;
    const float nm = -mx * SC;
    float sum = 0.f;
#pragma unroll
    for (int sl = 0; sl < 8; ++sl) { lg[sl] = __builtin_amdgcn_exp2f(__builtin_fmaf(lg[sl], SC, nm)); sum += lg[sl]; }
    const float rs = __builtin_amdgcn_rcpf(sum);
    unsigned u[4];
#pragma unroll
    for (int i = 0; i < 4; ++i) u[i] = pk2(lg[2 * i] * rs, lg[2 * i + 1] * rs);
    f32x4 O[5];
#pragma unroll
    for (int d = 0; d < 5; ++d) O[d] = mf4(VT[d][1][0], VT[d][1][1], u[2], u[3], z4);
#pragma unroll
    for (int d = 0; d < 5; ++d) O[d] = mf4(VT[d][0][0], VT[d][0][1], u[0] & m0, u[1] & m1, O[d]);
#pragma unroll
    for (int d = 0; d < 5; ++d) O[d] = mf4(VT[d][2][0], VT[d][2][1], u[0] & ~m0, u[1] & ~m1, O[d]);
    om[0] = u32x4{pk2(O[0][0], O[0][1]), pk2(O[0][2], O[0][3]), pk2(O[1][0], O[1][1]), pk2(O[1][2], O[1][3])};
    om[1] = u32x4{pk2(O[2][0], O[2][1]), pk2(O[2][2], O[2][3]), pk2(O[3][0], O[3][1]), pk2(O[3][2], O[3][3])};
    swap4(om[0], om[1]);
    ox[0] = pk2(O[4][2], O[4][3]); ox[1] = ox[0];
    swap16(ox[0], ox[1]);
  }
  STAMP(16); RSTAMP(27);
  lane = opq(tid) & 63; g = lane >> 4; c = lane & 15; tq = (lane >> 2) & 3; tp = lane & 3;
  obm = obt + 2 * wid * 18432 + hp * 1024 + lane * 16;
  wrow = (16 * (g >> 1) + 4 * (g & 1) + tq) * 160;
  unit = 16 * (tp & 1) + 40 * (tp >> 1);
  {
    const int obr = g == 1 ? obt + 2 * wid * 18432 + 16384 + hp * 128 + c * 8 : OOB;
#pragma unroll
    for (int mt = 0; mt < 2; ++mt) __builtin_amdgcn_raw_buffer_store_b128(canon4(om[mt]), wsr, obm + mt * 18432, 0, 16);
#pragma unroll
    for (int mt = 0; mt < 2; ++mt) {
      unsigned t0 = ox[mt], t1 = t0;
      swap32(t0, t1);
      __builtin_amdgcn_raw_buffer_store_b64(u32x2{canon(t0), canon(t1)}, wsr, obr + mt * 18432, 0, 16);
    }
  }
  STAMP(17);

  {
    const int tile = hf * 8 + wid;
    const int ob = W_OB + ((br * 8 + rto) * 16 + tile) * 18432;
    u32x4 of[18];
    const int oa_m = ob + lane * 16, oa_r = ob + 16384 + 2 * g * 128 + c * 8;
    auto ldL = [&]() {
#pragma unroll
      for (int i = 0; i < 2; ++i) {
        u32x2 a = __builtin_amdgcn_raw_buffer_load_b64(wsr, oa_r + i * 1024, 0, 16);
        u32x2 b = __builtin_amdgcn_raw_buffer_load_b64(wsr, oa_r + i * 1024 + 128, 0, 16);
        of[16 + i] = u32x4{a[0], a[1], b[0], b[1]};
      }
    };
    auto bad = [&](int i) {
      unsigned m = umax3(of[i][0], of[i][1], of[i][2]);
      m = m > of[i][3] ? m : of[i][3];
      return __any(m == SENT);
    };
    const int oa = wrow + unit + (ISA ? L_WO : L_WOB);
    const int os = wrow + ((tp & 1) ? 13 : 4) * 8 + (ISA ? L_WO : L_WOB);
    f32x4 C[5];
    bf16x8 wg[3][5];
    if ((ISA ? CSLA : CSLB) > 0) __builtin_amdgcn_s_sleep(ISA ? CSLA : CSLB);
    ldL();
    bar();
#pragma unroll 1
    for (int spin = 0; spin < SPIN_CAP; ++spin) {
#pragma unroll
      for (int i = 0; i < GD; ++i) of[i] = __builtin_amdgcn_raw_buffer_load_b128(wsr, oa_m, i * 1024, 16);
      SB();
#pragma unroll
      for (int t = 0; t < 5; ++t) C[t] = f32x4{0.f, 0.f, 0.f, 0.f};
      const int oa_l = opq(oa), os_l = opq(os), oa_h = opq(oa + 46080), os_h = opq(os + 46080);
      auto rdO = [&](auto J, auto T) {
        constexpr int kk = (J + 16) % 18, t = T, no = (kk % 9) * 5120;
        if constexpr (t < 4) return trfrag(L, (kk < 9 ? oa_l : oa_h) + no + (t >> 1) * 72 + (t & 1) * 8);
        else return trfrag(L, (kk < 9 ? os_l : os_h) + no);
      };
      sfor<5>([&](auto T) { wg[0][T] = rdO(I0{}, T); });
      sfor<5>([&](auto T) { wg[1][T] = rdO(std::integral_constant<int, 1>{}, T); });
      SB();
      if (bad(16) | bad(17)) {
#pragma unroll 1
        for (int sp2 = 0; sp2 < SPIN_CAP; ++sp2) {
          __builtin_amdgcn_s_sleep(GSLEEP);
          ldL();
          if (!(bad(16) | bad(17))) break;
        }
#pragma unroll
        for (int i = 0; i < GD; ++i)
          if (bad(i)) of[i] = __builtin_amdgcn_raw_buffer_load_b128(wsr, oa_m, i * 1024, 16);
        __builtin_amdgcn_s_waitcnt(0x0F70);
      }
      STAMP(18); RSTAMP(28);
      unsigned m = 0;
      sfor<18>([&](auto J) {
        constexpr int j = J, cur = j % 3, nxt = (j + 2) % 3, kk = (j + 16) % 18;
        sfor<5>([&](auto T) {
          SB();
          C[T] = mfma(wg[cur][T], asb(of[kk]), C[T]);
          if constexpr (j < 16) wg[nxt][T] = rdO(std::integral_constant<int, j + 2>{}, T);
          if constexpr (T == 0 && GD + j < 16) of[GD + j] = __builtin_amdgcn_raw_buffer_load_b128(wsr, oa_m, (GD + j) * 1024, 16);
          if constexpr (T == 1 && kk < 16) m = umax3(m, of[kk][0], of[kk][1]);
          if constexpr (T == 2 && kk < 16) m = umax3(m, of[kk][2], of[kk][3]);
        });
        SB();
      });
      if (__all(m != SENT)) break;
    }
    STAMP(19); RSTAMP(29);
    const int row = rto * 256 + 32 * (tile >> 1) + 2 * c + (tile & 1);
    __amdgpu_buffer_rsrc_t orr = __builtin_amdgcn_make_buffer_rsrc(OUT, 0, 2048 * 2304, 0x00020000);
    const int o = row * 2304 + br * 1152 + cg * 144;
    const int cu = 16 * (g & 1) + 40 * (g >> 1);
    __builtin_amdgcn_raw_buffer_store_b128(u32x4{pk2(C[0][0], C[0][1]), pk2(C[0][2], C[0][3]), pk2(C[1][0], C[1][1]), pk2(C[1][2], C[1][3])}, orr, o + cu, 0, OAUX);
    __builtin_amdgcn_raw_buffer_store_b128(u32x4{pk2(C[2][0], C[2][1]), pk2(C[2][2], C[2][3]), pk2(C[3][0], C[3][1]), pk2(C[3][2], C[3][3])}, orr, o + cu + 72, 0, OAUX);
    __builtin_amdgcn_raw_buffer_store_b64(u32x2{pk2(C[4][0], C[4][1]), pk2(C[4][2], C[4][3])}, orr, g < 2 ? o + (g ? 13 : 4) * 8 : OOB, 0, OAUX);
  }
}

__global__ __launch_bounds__(768, 3) void fused(Params P) {
  extern __shared__ __attribute__((aligned(16))) char lds_raw[];
  LAS char* L = (LAS char*)lds_raw;
  const int b = blockIdx.x, xcd = b & 7, br = b >> 7, jx = (b >> 3) & 15;
  u64 pc;
  asm volatile("s_getpc_b64 %0" : "=s"(pc));
  if (CPF >= 3) {
    const unsigned ix = (b >> 3) * 12 + (threadIdx.x >> 6);
    const unsigned iy = CPF == 3 ? ix : 332 - ix;
    warm1(pc & ~(u64)255, (iy < 332u ? iy : 332u) * 128);
    SB();
  }
  const char* XA = P.x[0];
  const char* WA = P.w[0];
  const char* X = br ? P.x[1] : P.x[0];
  const char* W = br ? P.w[1] : P.w[0];
  const char* WO = br ? P.wo[1] : P.wo[0];
  const char* X1 = P.x[1];
  const char* W1 = P.w[1];
  const char* PHI = P.wphi;
  char* OUT = P.out;
  char* WS = P.ws;
  asm volatile("" : "+s"(X), "+s"(W), "+s"(WO), "+s"(PHI), "+s"(OUT), "+s"(WS), "+s"(XA), "+s"(WA), "+s"(X1), "+s"(W1));
  u64 tag = 0;
  const int wid = __builtin_amdgcn_readfirstlane(threadIdx.x >> 6);
  if (wid >= 8) {
    if (br == 0) loader<true>(X, W, WO, X1, W1, WS, L, xcd, jx, wid);
    else loader<false>(X, W, WO, X1, W1, WS, L, xcd, jx, wid);
    return;
  }
  if (CPF && CPF < 3 && (CPF == 2 || br) && wid == 7) {
    const int ln = threadIdx.x & 63;
    if (CPF == 2) warm1(pc & ~(u64)255, (unsigned)(((br * 16 + jx) * 11 + (ln < 10 ? ln : 10)) * 128));
    else warm1(pc & ~(u64)255, (unsigned)((jx * 22 + (ln < 21 ? ln : 21)) * 128));
  }
  if (br == 0) body<true>(X, W, X1, W1, PHI, OUT, WS, L, xcd, jx, tag);
  else body<false>(XA, WA, X, W, PHI, OUT, WS, L, xcd, jx, tag);
  if (b == 255 && threadIdx.x < 64) {
    const GAS u64* f = (const GAS u64*)(WS + W_F) + threadIdx.x;
#pragma unroll 1
    for (int spin = 0; spin < SPIN_CAP; ++spin) {
      bool e = true;
#pragma unroll
      for (int i = 0; i < 4; ++i) e &= __hip_atomic_load(f + 64 * i, __ATOMIC_RELAXED, __HIP_MEMORY_SCOPE_AGENT) == tag;
      if (__all(e)) break;
      __builtin_amdgcn_s_sleep(2);
    }
    if (threadIdx.x == 0) __hip_atomic_store((GAS u64*)(WS + W_EPOCH), tag, __ATOMIC_RELAXED, __HIP_MEMORY_SCOPE_AGENT);
  }
}

extern "C" void kernel_launch(void* const* d_in, const int* in_sizes, int n_in, void* d_out, int out_size, void* d_ws,
                              size_t ws_size, hipStream_t stream) {
  Params P;
  P.x[0] = (const char*)d_in[0]; P.w[0] = (const char*)d_in[1]; P.wphi = (const char*)d_in[2]; P.wo[0] = (const char*)d_in[3];
  P.x[1] = (const char*)d_in[4]; P.w[1] = (const char*)d_in[5]; P.wo[1] = (const char*)d_in[6];
  P.out = (char*)d_out; P.ws = (char*)d_ws;
  (void)hipFuncSetAttribute((const void*)fused, hipFuncAttributeMaxDynamicSharedMemorySize, LDS_BYTES);
  hipLaunchKernelGGL(fused, dim3(256), dim3(768), LDS_BYTES, stream, P);
}
```
